```python
import torch

def _softmax(x, axis=-1):
    return torch.softmax(x, dim=axis)
def _gelu(x):
    return torch.nn.functional.gelu(x, approximate="tanh")
def _silu(x):
    return torch.nn.functional.silu(x)
def _sigmoid(x):
    return torch.sigmoid(x)
def _layernorm(x, axis=-1, eps=1e-5):
    m = x.mean(dim=axis, keepdim=True)
    v = x.var(dim=axis, keepdim=True, unbiased=False)
    return (x - m) / torch.sqrt(v + eps)
def _scatter_add(tgt, idx, src, axis=0):
    return tgt.clone().index_add_(axis, idx, src)
def _segment_sum(src, seg, n):
    out = torch.zeros((n,) + src.shape[1:], dtype=src.dtype)
    return out.index_add_(0, seg, src)
def _pad(x, pw):
    flat = tuple(v for pair in reversed(pw) for v in pair)
    return torch.nn.functional.pad(x, flat)

kernel_name = 'standalone__gqa_block_cached__sampling'

def setup_inputs():

    _w = (lambda a: (lambda t: t.to(torch.bfloat16) if t.is_floating_point() else t)(torch.as_tensor(a)))
    B, Sq, D = 8, 1, 448
    Hq, Hkv, Sk, Dh = 16, 8, 8192, 256
    assert Hq % Hkv == 0, (Hq, Hkv)
    return {
        "x": _w((lambda *s: torch.randn(*s, dtype=torch.bfloat16) * 0.8)(B, Sq, D)),
        "k_cache": _w((lambda *s: torch.randn(*s, dtype=torch.bfloat16) * 0.8)(B, Hkv, Sk, Dh)),
        "v_cache": _w((lambda *s: torch.randn(*s, dtype=torch.bfloat16) * 0.05)(B, Hkv, Sk, Dh)),
        "wq": _w((lambda *s: torch.randn(*s, dtype=torch.bfloat16) * 0.8)(D, Hq * Dh) * (D ** -0.5)),
        "wo": _w((lambda *s: torch.randn(*s, dtype=torch.bfloat16) * 0.05)(Hq * Dh, D) * ((Hq * Dh) ** -0.5)),
    }

def reference(x, k_cache, v_cache, wq, wo):
    B, Sq, D = x.shape
    Hq, Dh = 16, 256
    Hkv = k_cache.shape[1]
    rep = Hq // Hkv
    q = (lambda x, *d: x.permute(*d))((x @ wq).reshape(B, Sq, Hq, Dh), 0, 2, 1, 3)
    k = (lambda x, n, axis: torch.repeat_interleave(x, n, dim=axis))(k_cache, rep, axis=1)
    v = (lambda x, n, axis: torch.repeat_interleave(x, n, dim=axis))(v_cache, rep, axis=1)
    scores = torch.einsum("bhqd,bhkd->bhqk", q, k) * (Dh ** -0.5)
    p = _softmax(scores, axis=-1)
    o = torch.einsum("bhqk,bhkd->bhqd", p, v)
    return (lambda x, *d: x.permute(*d))(o, 0, 2, 1, 3).reshape(B, Sq, Hq * Dh) @ wo
```

```cpp
#include <hip/hip_runtime.h>
#include <hip/hip_bf16.h>

typedef unsigned v4u __attribute__((ext_vector_type(4)));
typedef float f32x4 __attribute__((ext_vector_type(4)));
typedef __bf16 bf16x2 __attribute__((ext_vector_type(2)));

constexpr int B = 8, DM = 448, HQ = 16, HKV = 8, SK = 8192, DH = 256;
constexpr int NSPLIT = 4;
constexpr int KEYS_PER_WG = SK / NSPLIT;

__device__ __forceinline__ float bf2f(unsigned short v) { return __builtin_bit_cast(float, (unsigned)v << 16); }
__device__ __forceinline__ float dot2(unsigned k, unsigned q, float acc) {
  return __builtin_amdgcn_fdot2_f32_bf16(__builtin_bit_cast(bf16x2, k), __builtin_bit_cast(bf16x2, q), acc, false);
}
__device__ __forceinline__ unsigned short f2bf(float f) { __hip_bfloat16 h = __float2bfloat16(f); return __builtin_bit_cast(unsigned short, h); }

__global__ void __launch_bounds__(256) k_qproj(const unsigned short* __restrict__ x, const unsigned short* __restrict__ wq, unsigned short* __restrict__ q) {
  __shared__ float xs[B][DM];
  __shared__ float red[4][B][64];
  const int tid = threadIdx.x;
  for (int i = tid; i < B * DM; i += 256) xs[i / DM][i % DM] = bf2f(x[i]);
  __syncthreads();
  const int col = blockIdx.x * 64 + (tid & 63), w = tid >> 6;
  float acc[B];
#pragma unroll
  for (int b = 0; b < B; ++b) acc[b] = 0.f;
  for (int k = w * 112; k < (w + 1) * 112; ++k) {
    const float wv = bf2f(wq[(size_t)k * 4096 + col]);
#pragma unroll
    for (int b = 0; b < B; ++b) acc[b] = fmaf(xs[b][k], wv, acc[b]);
  }
#pragma unroll
  for (int b = 0; b < B; ++b) red[w][b][tid & 63] = acc[b];
  __syncthreads();
  for (int i = tid; i < B * 64; i += 256) {
    const int b = i >> 6, c = i & 63;
    const float s = red[0][b][c] + red[1][b][c] + red[2][b][c] + red[3][b][c];
    q[b * 4096 + blockIdx.x * 64 + c] = f2bf(s);
  }
}

__global__ void __launch_bounds__(256) k_attn(const v4u* __restrict__ K, const v4u* __restrict__ V, const v4u* __restrict__ q, float* __restrict__ po, float* __restrict__ pml) {
  __shared__ float mrg[4][2][2 + DH + 2];
  const int tid = threadIdx.x, lane = tid & 63, wave = __builtin_amdgcn_readfirstlane(tid >> 6);
  const int bid = blockIdx.x;
  const int s = bid & 3, bh = bid >> 2;
  const int b = bh >> 3, kvh = bh & 7;
  const int c = lane & 31, r = lane >> 5;
  const v4u q0 = q[(b * 4096 + (kvh * 2 + 0) * 256) / 8 + c];
  const v4u q1 = q[(b * 4096 + (kvh * 2 + 1) * 256) / 8 + c];
  constexpr int U = 4;
  constexpr float C2 = 0.0625f * 1.4426950408889634f;
  const size_t base = ((size_t)bh * SK + (size_t)s * KEYS_PER_WG) * 32 + (size_t)wave * 64 * U + lane;
  const v4u* kp = K + base; const v4u* vp = V + base;
  constexpr int NT = KEYS_PER_WG / (2 * U * 4);
  v4u kn[U], vn[U];
#pragma unroll
  for (int u = 0; u < U; ++u) kn[u] = __builtin_nontemporal_load(kp + u * 64);
#pragma unroll
  for (int u = 0; u < U; ++u) vn[u] = __builtin_nontemporal_load(vp + u * 64);
  float m0 = -INFINITY, m1 = -INFINITY, l0 = 0.f, l1 = 0.f;
  float a0[8], a1[8];
#pragma unroll
  for (int j = 0; j < 8; ++j) { a0[j] = 0.f; a1[j] = 0.f; }
  for (int t = 0; t < NT; ++t) {
    float s0[U], s1[U];
#pragma unroll
    for (int u = 0; u < U; ++u) {
      float d0 = 0.f, d1 = 0.f;
#pragma unroll
      for (int j = 0; j < 4; ++j) { d0 = dot2(kn[u][j], q0[j], d0); d1 = dot2(kn[u][j], q1[j], d1); }
      s0[u] = d0; s1[u] = d1;
    }
    kp += 4 * 64 * U;
    if (t + 1 < NT) {
#pragma unroll
      for (int u = 0; u < U; ++u) kn[u] = __builtin_nontemporal_load(kp + u * 64);
    }
#pragma unroll
    for (int u = 0; u < U; ++u) {
#pragma unroll
      for (int o = 16; o >= 1; o >>= 1) { s0[u] += __shfl_xor(s0[u], o); s1[u] += __shfl_xor(s1[u], o); }
      s0[u] *= C2; s1[u] *= C2;
    }
    float mx0 = m0, mx1 = m1;
#pragma unroll
    for (int u = 0; u < U; ++u) { mx0 = fmaxf(mx0, s0[u]); mx1 = fmaxf(mx1, s1[u]); }
    const float al0 = __builtin_amdgcn_exp2f(m0 - mx0), al1 = __builtin_amdgcn_exp2f(m1 - mx1);
    m0 = mx0; m1 = mx1;
    l0 *= al0; l1 *= al1;
#pragma unroll
    for (int j = 0; j < 8; ++j) { a0[j] *= al0; a1[j] *= al1; }
#pragma unroll
    for (int u = 0; u < U; ++u) {
      const float p0 = __builtin_amdgcn_exp2f(s0[u] - m0), p1 = __builtin_amdgcn_exp2f(s1[u] - m1);
      l0 += p0; l1 += p1;
#pragma unroll
      for (int j = 0; j < 4; ++j) {
        const float lo = __builtin_bit_cast(float, vn[u][j] << 16), hi = __builtin_bit_cast(float, vn[u][j] & 0xffff0000u);
        a0[2 * j] = fmaf(p0, lo, a0[2 * j]); a0[2 * j + 1] = fmaf(p0, hi, a0[2 * j + 1]);
        a1[2 * j] = fmaf(p1, lo, a1[2 * j]); a1[2 * j + 1] = fmaf(p1, hi, a1[2 * j + 1]);
      }
    }
    vp += 4 * 64 * U;
    if (t + 1 < NT) {
#pragma unroll
      for (int u = 0; u < U; ++u) vn[u] = __builtin_nontemporal_load(vp + u * 64);
    }
  }
  {
    const float om0 = __shfl_xor(m0, 32), om1 = __shfl_xor(m1, 32);
    const float M0 = fmaxf(m0, om0), M1 = fmaxf(m1, om1);
    const float w0 = __builtin_amdgcn_exp2f(m0 - M0), w1 = __builtin_amdgcn_exp2f(m1 - M1);
    l0 *= w0; l1 *= w1;
    l0 += __shfl_xor(l0, 32); l1 += __shfl_xor(l1, 32);
#pragma unroll
    for (int j = 0; j < 8; ++j) { a0[j] *= w0; a1[j] *= w1; a0[j] += __shfl_xor(a0[j], 32); a1[j] += __shfl_xor(a1[j], 32); }
    m0 = M0; m1 = M1;
  }
  if (r == 0) {
#pragma unroll
    for (int j = 0; j < 8; ++j) { mrg[wave][0][4 + 8 * c + j] = a0[j]; mrg[wave][1][4 + 8 * c + j] = a1[j]; }
    if (c == 0) { mrg[wave][0][0] = m0; mrg[wave][0][1] = l0; mrg[wave][1][0] = m1; mrg[wave][1][1] = l1; }
  }
  __syncthreads();
#pragma unroll
  for (int h = 0; h < 2; ++h) {
    float M = -INFINITY;
#pragma unroll
    for (int w = 0; w < 4; ++w) M = fmaxf(M, mrg[w][h][0]);
    float L = 0.f, o = 0.f;
#pragma unroll
    for (int w = 0; w < 4; ++w) { const float wt = __builtin_amdgcn_exp2f(mrg[w][h][0] - M); L = fmaf(wt, mrg[w][h][1], L); o = fmaf(wt, mrg[w][h][4 + tid], o); }
    const int row = (b * HQ + kvh * 2 + h) * NSPLIT + s;
    po[(size_t)row * DH + tid] = o;
    if (tid == 0) { pml[row * 2] = M; pml[row * 2 + 1] = L; }
  }
}

__global__ void __launch_bounds__(1024) k_out(const float* __restrict__ po, const float* __restrict__ pml, const unsigned short* __restrict__ wo, unsigned short* __restrict__ out) {
  __shared__ float os[4096];
  __shared__ float red[16][64];
  const int tid = threadIdx.x, b = blockIdx.x / 7, nc = blockIdx.x % 7;
  for (int i = tid; i < 4096; i += 1024) {
    const int h = i >> 8, d = i & 255;
    const int row = (b * HQ + h) * NSPLIT;
    float M = -INFINITY;
#pragma unroll
    for (int s = 0; s < NSPLIT; ++s) M = fmaxf(M, pml[(row + s) * 2]);
    float L = 0.f, o = 0.f;
#pragma unroll
    for (int s = 0; s < NSPLIT; ++s) { const float wt = __builtin_amdgcn_exp2f(pml[(row + s) * 2] - M); L = fmaf(wt, pml[(row + s) * 2 + 1], L); o = fmaf(wt, po[(size_t)(row + s) * DH + d], o); }
    os[i] = bf2f(f2bf(o / L));
  }
  __syncthreads();
  const int w = tid >> 6, col = nc * 64 + (tid & 63);
  float acc = 0.f;
#pragma unroll 16
  for (int k = w * 256; k < (w + 1) * 256; ++k) acc = fmaf(os[k], bf2f(wo[(size_t)k * DM + col]), acc);
  red[w][tid & 63] = acc;
  __syncthreads();
  if (tid < 64) {
    float s = 0.f;
#pragma unroll
    for (int i = 0; i < 16; ++i) s += red[i][tid];
    out[b * DM + nc * 64 + tid] = f2bf(s);
  }
}

extern "C" void kernel_launch(void* const* d_in, const int* in_sizes, int n_in, void* d_out, int out_size, void* d_ws, size_t ws_size, hipStream_t stream) {
  const unsigned short* x = (const unsigned short*)d_in[0];
  const v4u* K = (const v4u*)d_in[1];
  const v4u* V = (const v4u*)d_in[2];
  const unsigned short* wq = (const unsigned short*)d_in[3];
  const unsigned short* wo = (const unsigned short*)d_in[4];
  unsigned char* ws = (unsigned char*)d_ws;
  unsigned short* q = (unsigned short*)ws;
  float* pml = (float*)(ws + 65536);
  float* po = (float*)(ws + 65536 + 8192);
  k_qproj<<<64, 256, 0, stream>>>(x, wq, q);
  k_attn<<<B * HKV * NSPLIT, 256, 0, stream>>>(K, V, (const v4u*)q, po, pml);
  k_out<<<56, 1024, 0, stream>>>(po, pml, wo, (unsigned short*)d_out);
}
```

```cpp
#include <hip/hip_runtime.h>
#include <hip/hip_bf16.h>
#include <type_traits>

typedef unsigned v4u __attribute__((ext_vector_type(4)));
typedef unsigned v2u __attribute__((ext_vector_type(2)));
typedef float f32x4 __attribute__((ext_vector_type(4)));
typedef float f32x2 __attribute__((ext_vector_type(2)));
typedef short bf16x8 __attribute__((ext_vector_type(8)));
typedef short s16x4 __attribute__((ext_vector_type(4)));
typedef __bf16 bf16x2 __attribute__((ext_vector_type(2)));
#define LAS __attribute__((address_space(3)))
#define GAS __attribute__((address_space(1)))

#define RS 3
#define NIF 5
#define NIFA NIF

constexpr int NB = 8, DM = 448, HKV = 8, SK = 8192, DH = 256, NQ = 4096;
constexpr float C2 = 0.0625f * 1.4426950408889634f;
constexpr unsigned TAG = 0x5A5A0001u;
constexpr unsigned SPIN_CAP = 1u << 20;

constexpr size_t W_Q = 0;
constexpr size_t W_FLAG = 65536;
constexpr size_t W_INBOX = W_FLAG + 65536;
constexpr size_t W_GP = W_INBOX + 256 * 4096;
constexpr size_t W_GM = W_GP + 64 * 32768;
constexpr size_t W_END = W_GM + 64 * 2048;

constexpr int L_RING = 0;
constexpr int L_P = 4 * RS * 8192;
constexpr int L_MREF = L_P + 32768;
constexpr int L_Q = L_MREF + 2048;
constexpr int L_MRG = L_Q + 1024;
constexpr int L_ST = L_MRG + 2048;
constexpr int L_FLG = L_ST + 64;
constexpr int L_END = L_FLG + 64;

template <int N> __device__ __forceinline__ void waitvm() { asm volatile("s_waitcnt vmcnt(%0)" ::"i"(N) : "memory"); }
__device__ __forceinline__ void waitlgkm() { asm volatile("s_waitcnt lgkmcnt(0)" ::: "memory"); }
__device__ __forceinline__ void bar() { asm volatile("s_waitcnt lgkmcnt(0)\n\ts_barrier" ::: "memory"); }
__device__ __forceinline__ void dma_nt(unsigned ldsaddr, unsigned voff, const void* sbase) {
  unsigned sv;
  asm volatile("s_mov_b32 %0, m0\n\ts_mov_b32 m0, %1\n\ts_nop 0\n\tglobal_load_lds_dwordx4 %2, %3 nt\n\ts_mov_b32 m0, %0" : "=&s"(sv) : "s"(ldsaddr), "v"(voff), "s"(sbase) : "memory");
}
__device__ __forceinline__ void dma_def(unsigned ldsaddr, unsigned voff, const void* sbase) {
  unsigned sv;
  asm volatile("s_mov_b32 %0, m0\n\ts_mov_b32 m0, %1\n\ts_nop 0\n\tglobal_load_lds_dwordx4 %2, %3\n\ts_mov_b32 m0, %0" : "=&s"(sv) : "s"(ldsaddr), "v"(voff), "s"(sbase) : "memory");
}
__device__ __forceinline__ f32x4 mfma(v4u a, v4u b, f32x4 c) {
  return __builtin_amdgcn_mfma_f32_16x16x32_bf16(__builtin_bit_cast(bf16x8, a), __builtin_bit_cast(bf16x8, b), c, 0, 0, 0);
}
__device__ __forceinline__ v2u ldtr(const LAS char* p) {
  return __builtin_bit_cast(v2u, __builtin_amdgcn_ds_read_tr16_b64_v4i16((LAS s16x4*)p));
}
__device__ __forceinline__ unsigned cvtpk(float lo, float hi) { return __builtin_bit_cast(unsigned, __builtin_convertvector(f32x2{lo, hi}, bf16x2)); }
__device__ __forceinline__ float dot2(unsigned a, unsigned b, float acc) {
  return __builtin_amdgcn_fdot2_f32_bf16(__builtin_bit_cast(bf16x2, a), __builtin_bit_cast(bf16x2, b), acc, false);
}
__device__ __forceinline__ float asf(unsigned u) { return __builtin_bit_cast(float, u); }
__device__ __forceinline__ unsigned asu(float f) { return __builtin_bit_cast(unsigned, f); }
__device__ __forceinline__ float rows_max(float x) {
  auto s = __builtin_amdgcn_permlane16_swap(asu(x), asu(x), false, false);
  x = fmaxf(asf(s[0]), asf(s[1]));
  auto t = __builtin_amdgcn_permlane32_swap(asu(x), asu(x), false, false);
  return fmaxf(asf(t[0]), asf(t[1]));
}
__device__ __forceinline__ float rows_sum(float x) {
  auto s = __builtin_amdgcn_permlane16_swap(asu(x), asu(x), false, false);
  x = asf(s[0]) + asf(s[1]);
  auto t = __builtin_amdgcn_permlane32_swap(asu(x), asu(x), false, false);
  return asf(t[0]) + asf(t[1]);
}
template <int N> __device__ __forceinline__ unsigned row_bcast(unsigned x) { return (unsigned)__builtin_amdgcn_update_dpp(0, (int)x, 0x150 + N, 0xf, 0xf, false); }
#define RSRC(p, n) __builtin_amdgcn_make_buffer_rsrc((void*)(p), 0, (int)(n), 0x00020000)
constexpr int SC1 = 16;
#define EXST_AUX 16
#define EXLD_AUX 16
#define STAMP(i) do {} while (0)
#define STAMP_FLUSH() do {} while (0)

__global__ void __launch_bounds__(256) k_q(const char* __restrict__ x, const char* __restrict__ wq, char* __restrict__ ws) {
  __shared__ __attribute__((aligned(16))) char lds[14 * 1024 + 4096];
  const int tid = threadIdx.x, lane = tid & 63, wave = __builtin_amdgcn_readfirstlane(tid >> 6);
  const int j = blockIdx.x, cb = (j & 7) * 32 + (j >> 3);
  const int g = lane >> 4, c = lane & 15;
  const unsigned l0 = (unsigned)(size_t)(LAS char*)lds;
  const unsigned voff = (lane >> 1) * 8192 + (lane & 1) * 16;
#pragma unroll
  for (int i = 0; i < 4; ++i) {
    const int s = wave + 4 * i;
    if (s < 14) dma_def(l0 + s * 1024, voff, wq + (size_t)s * 32 * 8192 + cb * 32);
  }
  v4u xf[4];
#pragma unroll
  for (int i = 0; i < 4; ++i) {
    const int s = (wave + 4 * i < 14) ? wave + 4 * i : 0;
    xf[i] = *(const GAS v4u*)(x + (c & 7) * (DM * 2) + s * 64 + g * 16);
  }
  *(GAS v4u*)(ws + W_INBOX + (size_t)j * 4096 + tid * 16) = v4u{0, 0, 0, 0};
  if (tid == 0) *(GAS v4u*)(ws + W_FLAG + (size_t)j * 256) = v4u{0, 0, 0, 0};
  waitvm<0>();
  f32x4 acc = {0.f, 0.f, 0.f, 0.f};
  const LAS char* lp = (const LAS char*)lds + (8 * g + ((lane >> 2) & 3)) * 32 + (lane & 3) * 8;
#pragma unroll
  for (int i = 0; i < 4; ++i) {
    const int s = wave + 4 * i;
    if (s < 14) {
      const v2u a0 = ldtr(lp + s * 1024), a1 = ldtr(lp + s * 1024 + 128);
      acc = mfma(v4u{a0.x, a0.y, a1.x, a1.y}, xf[i], acc);
    }
  }
  *(LAS f32x4*)((LAS char*)lds + 14 * 1024 + wave * 1024 + lane * 16) = acc;
  bar();
  if (wave == 0 && c < 8) {
    const LAS f32x4* r = (const LAS f32x4*)((LAS char*)lds + 14 * 1024 + lane * 16);
    const f32x4 s = (r[0] + r[64]) + (r[128] + r[192]);
    *(GAS v2u*)(ws + W_Q + (size_t)c * (NQ * 2) + (cb * 16 + 4 * g) * 2) = v2u{cvtpk(s[0], s[1]), cvtpk(s[2], s[3])};
  }
}

__global__ void __launch_bounds__(512, 2) k_main(const char* __restrict__ Kc, const char* __restrict__ Vc, const char* __restrict__ wo, char* __restrict__ ws, unsigned* __restrict__ out) {
  extern __shared__ __attribute__((aligned(16))) char lds_[];
  LAS char* lds = (LAS char*)lds_;
  const int tid = threadIdx.x, lane = tid & 63, wave = __builtin_amdgcn_readfirstlane(tid >> 6);
  const int bid = blockIdx.x, grp = bid >> 2, m = bid & 3, b = bid >> 5, jb = bid & 31, kvh = jb >> 2;
  const int g = lane >> 4, c = lane & 15;
  volatile LAS unsigned* flg = (volatile LAS unsigned*)(lds + L_FLG);
  STAMP(0);
  if (wave < 4) {
    __builtin_amdgcn_s_setprio(3);
    const char* kb = Kc + (size_t)grp * (SK * DH * 2) + (size_t)(m * 4 + wave) * 8192;
    const char* vb = Vc + (size_t)grp * (SK * DH * 2) + (size_t)wave * 16384;
    const unsigned ring0 = (unsigned)(size_t)lds + L_RING + wave * (RS * 8192);
    const unsigned kvo = (lane >> 5) * 512 + (((lane & 31) ^ (lane >> 5)) * 16);
    const unsigned vvo = (lane >> 3) * 512 + m * 128 + ((((lane & 7) >> 1) ^ ((lane >> 4) & 3)) * 32) + (lane & 1) * 16;
    auto srcof = [&](int s) -> const char* { return s < 32 ? kb + (size_t)s * 131072 : vb + (size_t)((s - 32) >> 1) * 262144 + (s & 1) * 65536; };
#define ISS_K(p) do { waitvm<NIFA - 1>(); dma_nt(dst + (p) * 1024, kvo ^ ((p) << 5), src + (p) * 1024); } while (0)
#define ISS_V(p) do { waitvm<NIF - 1>(); dma_nt(dst + (p) * 1024, vvo, src + ((p) & 3) * 4096 + ((p) >> 2) * 131072); } while (0)
#define ISS(MODE, p) do { if (MODE == 0) ISS_K(p); else if (MODE == 1) ISS_V(p); } while (0)
#pragma unroll
    for (int s = 0; s < RS; ++s) {
      const unsigned dst = ring0 + s * 8192; const char* src = srcof(s);
#pragma unroll
      for (int p = 0; p < 8; ++p) ISS_K(p);
    }
    bar();
    STAMP(1);
    v4u qf[8];
#pragma unroll
    for (int s = 0; s < 8; ++s) qf[s] = *(const LAS v4u*)(lds + L_Q + (c & 1) * 512 + s * 64 + g * 16);
    const LAS char* wring = lds + L_RING + wave * (RS * 8192);
    const int kro = c * 512 + ((g ^ c) * 16);
    auto qk = [&](auto mode, int cs, int is_, int fs) -> f32x4 {
      constexpr int MODE = decltype(mode)::value;
      const LAS char* base = wring + cs * 8192;
      const unsigned dst = ring0 + fs * 8192; const char* src = srcof(is_);
      f32x4 a = {0.f, 0.f, 0.f, 0.f};
#pragma unroll
      for (int p = 0; p < 8; ++p) {
        ISS(MODE, p);
        const v4u kf = *(const LAS v4u*)(base + ((kro ^ ((p & 3) << 6)) + (p >> 2) * 256));
        a = mfma(kf, qf[p], a);
        asm volatile("" : "+v"(a));
      }
      return a;
    };
    auto softmax_pub = [&](const f32x4& d0, const f32x4& d1, int i) {
      float mx = fmaxf(fmaxf(fmaxf(d0[0], d0[1]), fmaxf(d0[2], d0[3])), fmaxf(fmaxf(d1[0], d1[1]), fmaxf(d1[2], d1[3])));
      mx = rows_max(mx) * C2;
      v4u pk;
      pk.x = cvtpk(__builtin_amdgcn_exp2f(fmaf(d0[0], C2, -mx)), __builtin_amdgcn_exp2f(fmaf(d0[1], C2, -mx)));
      pk.y = cvtpk(__builtin_amdgcn_exp2f(fmaf(d0[2], C2, -mx)), __builtin_amdgcn_exp2f(fmaf(d0[3], C2, -mx)));
      pk.z = cvtpk(__builtin_amdgcn_exp2f(fmaf(d1[0], C2, -mx)), __builtin_amdgcn_exp2f(fmaf(d1[1], C2, -mx)));
      pk.w = cvtpk(__builtin_amdgcn_exp2f(fmaf(d1[2], C2, -mx)), __builtin_amdgcn_exp2f(fmaf(d1[3], C2, -mx)));
      const int G = i * 16 + m * 4 + wave;
      if (c < 2) {
        *(LAS v4u*)(lds + L_P + G * 128 + (g * 2 + c) * 16) = pk;
        if (g == 0) *(LAS float*)(lds + L_MREF + G * 8 + c * 4) = mx;
      }
      if ((i & 7) == 7) { waitlgkm(); if (lane == 0) flg[wave] = i + 1; }
    };
    using M0_ = std::integral_constant<int, 0>; using M1_ = std::integral_constant<int, 1>; using M2_ = std::integral_constant<int, 2>;
    auto nxt = [](int x) { return x == RS - 1 ? 0 : x + 1; };
    waitvm<8 * (RS - 1)>();
    f32x4 d0 = qk(M2_{}, 0, 0, 0);
    int fs = 0, cs = 1;
    constexpr int NFULL = (32 - RS + 1) / 2;
    int i = 0;
    for (; i < NFULL - ((32 - RS + 1) % 2 == 0 ? 1 : 0); ++i) {
      waitvm<8 * (RS - 2)>();
      const f32x4 d1 = qk(M0_{}, cs, 2 * i + RS, fs);
      softmax_pub(d0, d1, i);
      fs = cs; cs = nxt(cs);
      waitvm<8 * (RS - 2)>();
      d0 = qk(M0_{}, cs, 2 * i + 1 + RS, fs);
      fs = cs; cs = nxt(cs);
    }
    for (; i < 16; ++i) {
      waitvm<8 * (RS - 2)>();
      const f32x4 d1 = (2 * i + RS < 32) ? qk(M0_{}, cs, 2 * i + RS, fs) : qk(M1_{}, cs, 2 * i + RS, fs);
      softmax_pub(d0, d1, i);
      fs = cs; cs = nxt(cs);
      if (i < 15) {
        waitvm<8 * (RS - 2)>();
        d0 = (2 * i + 1 + RS < 32) ? qk(M0_{}, cs, 2 * i + 1 + RS, fs) : qk(M1_{}, cs, 2 * i + 1 + RS, fs);
        fs = cs; cs = nxt(cs);
      }
    }
    STAMP(2);
    f32x4 acc[4];
#pragma unroll
    for (int d = 0; d < 4; ++d) acc[d] = f32x4{0.f, 0.f, 0.f, 0.f};
    float M = -INFINITY, L = 0.f;
    const int kq = 4 * g + ((lane >> 2) & 3);
    const int vro = kq * 128 + (((kq >> 1) & 3) * 32) + (lane & 3) * 8;
    const v4u ones = {0x3F803F80u, 0x3F803F80u, 0x3F803F80u, 0x3F803F80u};
    auto pv = [&](auto mode, int i) {
      constexpr int MODE = decltype(mode)::value;
      const LAS char* base = wring + cs * 8192;
      const unsigned dst = ring0 + fs * 8192; const char* src = srcof(32 + i + RS - 1);
      const int G0 = (i * 4 + wave) * 2;
#pragma unroll
      for (int h = 0; h < 2; ++h) {
        ISS(MODE, 4 * h);
        const v4u pf = *(const LAS v4u*)(lds + L_P + (G0 + h) * 128 + (g * 2 + (c & 1)) * 16);
        const float mr = *(const LAS float*)(lds + L_MREF + (G0 + h) * 8 + (c & 1) * 4);
        const float Mn = fmaxf(M, mr);
        const float al = __builtin_amdgcn_exp2f(M - Mn), be = __builtin_amdgcn_exp2f(mr - Mn);
        M = Mn;
        const f32x4 tl = mfma(ones, pf, f32x4{0.f, 0.f, 0.f, 0.f});
        L = L * al + tl[0] * be;
#pragma unroll
        for (int d = 0; d < 4; ++d) {
          if (d > 0) ISS(MODE, 4 * h + d);
          const LAS char* a = base + h * 2048 + (vro ^ (d << 5));
          const v2u a0 = ldtr(a), a1 = ldtr(a + 4096);
          const f32x4 t = mfma(v4u{a0.x, a0.y, a1.x, a1.y}, pf, f32x4{0.f, 0.f, 0.f, 0.f});
          acc[d] = acc[d] * al + t * be;
          if (MODE != 2) asm volatile("" : "+v"(acc[d]));
        }
      }
      fs = cs; cs = nxt(cs);
    };
    for (int i = 0; i < 32 - RS + 1; ++i) {
      if ((i & 15) == 0) {
        const unsigned need = (i >> 4) + 1;
        for (unsigned sp = 0; flg[4] < need && sp < SPIN_CAP; ++sp) __builtin_amdgcn_s_sleep(2);
        asm volatile("" ::: "memory");
      }
      waitvm<8 * (RS - 2)>();
      pv(M1_{}, i);
    }
#pragma unroll
    for (int i = 32 - RS + 1; i < 32; ++i) {
      if (i == 31) waitvm<0>(); else if (i == 30) waitvm<8>(); else waitvm<16>();
      pv(M2_{}, i);
    }
    if (c < 2) {
#pragma unroll
      for (int d = 0; d < 4; ++d) *(LAS f32x4*)(lds + L_MRG + wave * 512 + c * 256 + (16 * d + 4 * g) * 4) = acc[d];
      if (g == 0) *(LAS f32x2*)(lds + L_ST + wave * 16 + c * 8) = f32x2{M, L};
    }
    STAMP(3);
    bar();
    if (wave == 0 && jb < 28) {
      const __amdgpu_buffer_rsrc_t rs = RSRC(ws + W_INBOX + (size_t)bid * 4096, 4096);
      v4u v[4];
      for (unsigned sp = 0; sp < SPIN_CAP; ++sp) {
        asm volatile("" ::: "memory");
#pragma unroll
        for (int it = 0; it < 4; ++it) v[it] = __builtin_amdgcn_raw_buffer_load_b128(rs, it * 1024 + lane * 16, 0, SC1);
        bool ok = true;
#pragma unroll
        for (int it = 0; it < 4; ++it) ok &= (v[it].y == TAG) & (v[it].w == TAG);
        if (__all(ok)) break;
      }
      float s0 = (asf(v[0].x) + asf(v[1].x)) + (asf(v[2].x) + asf(v[3].x));
      float s1 = (asf(v[0].z) + asf(v[1].z)) + (asf(v[2].z) + asf(v[3].z));
      s0 += asf(__builtin_amdgcn_update_dpp(0, (int)asu(s0), 0x128, 0xf, 0xf, false));
      s1 += asf(__builtin_amdgcn_update_dpp(0, (int)asu(s1), 0x128, 0xf, 0xf, false));
      s0 = rows_sum(s0); s1 = rows_sum(s1);
      if (lane < 8) out[b * (DM / 2) + jb * 8 + lane] = cvtpk(s0, s1);
      STAMP(4);
    }
  } else {
    const int hw = wave - 4;
    if (hw == 0) {
      const v4u qv = *(const GAS v4u*)(ws + W_Q + (size_t)b * (NQ * 2) + kvh * 1024 + lane * 16);
      *(LAS v4u*)(lds + L_Q + lane * 16) = qv;
      if (lane < 16) flg[lane] = 0u;
    }
    bar();
    const int ch = c < 14 ? c : 13;
    const char* wp = wo + (size_t)((2 * kvh + (g >> 1)) * 256 + 64 * m + 32 * (g & 1)) * (DM * 2) + hw * 224 + ch * 16;
    unsigned wr[16][8];
#pragma unroll
    for (int i0 = 0; i0 < 16; i0 += 2) {
      v4u r[4];
#pragma unroll
      for (int u = 0; u < 4; ++u) r[u] = *(const GAS v4u*)(wp + (size_t)(2 * i0 + u) * (DM * 2));
#pragma unroll
      for (int u = 0; u < 2; ++u)
#pragma unroll
        for (int e = 0; e < 4; ++e) {
          wr[i0 + u][2 * e] = __builtin_amdgcn_perm(r[2 * u + 1][e], r[2 * u][e], 0x05040100u);
          wr[i0 + u][2 * e + 1] = __builtin_amdgcn_perm(r[2 * u + 1][e], r[2 * u][e], 0x07060302u);
        }
#pragma unroll
      for (int u = 0; u < 2; ++u)
        asm volatile("" : "+v"(wr[i0 + u][0]), "+v"(wr[i0 + u][1]), "+v"(wr[i0 + u][2]), "+v"(wr[i0 + u][3]), "+v"(wr[i0 + u][4]), "+v"(wr[i0 + u][5]), "+v"(wr[i0 + u][6]), "+v"(wr[i0 + u][7]) :: "memory");
    }
    if (hw == 0) {
      const __amdgpu_buffer_rsrc_t rp = RSRC(ws + W_GP + (size_t)grp * 32768, 32768);
      const __amdgpu_buffer_rsrc_t rm = RSRC(ws + W_GM + (size_t)grp * 2048, 2048);
#pragma unroll
      for (int ck = 0; ck < 2; ++ck) {
        const unsigned need = 8 * (ck + 1);
        for (unsigned sp = 0; sp < SPIN_CAP; ++sp) {
          if (flg[0] >= need && flg[1] >= need && flg[2] >= need && flg[3] >= need) break;
          __builtin_amdgcn_s_sleep(16);
        }
        asm volatile("" ::: "memory");
        if (ck == 0) STAMP(7);
#pragma unroll
        for (int it = 0; it < 4; ++it) {
          const int off = ((8 * ck + it * 2 + (lane >> 5)) * 16 + m * 4) * 128 + (lane & 31) * 16;
          __builtin_amdgcn_raw_buffer_store_b128(*(const LAS v4u*)(lds + L_P + off), rp, off, 0, EXST_AUX);
        }
        if (lane < 16) {
          const int off = ((8 * ck + (lane >> 1)) * 16 + m * 4) * 8 + (lane & 1) * 16;
          __builtin_amdgcn_raw_buffer_store_b128(*(const LAS v4u*)(lds + L_MREF + off), rm, off, 0, EXST_AUX);
        }
        waitvm<0>();
        STAMP(5 + ck);
        if (lane == 0) __hip_atomic_store((GAS unsigned*)(ws + W_FLAG + (size_t)bid * 256), (unsigned)(ck + 1), __ATOMIC_RELAXED, __HIP_MEMORY_SCOPE_AGENT);
        for (int pe = 1; pe < 4; ++pe) {
          const int mm = (m + pe) & 3;
          GAS unsigned* pf = (GAS unsigned*)(ws + W_FLAG + (size_t)(grp * 4 + mm) * 256);
          for (unsigned sp = 0; sp < SPIN_CAP; ++sp) {
            if (__hip_atomic_load(pf, __ATOMIC_RELAXED, __HIP_MEMORY_SCOPE_AGENT) >= (unsigned)(ck + 1)) break;
            __builtin_amdgcn_s_sleep(16);
          }
          __builtin_amdgcn_fence(__ATOMIC_ACQUIRE, "wavefront");
          asm volatile("" ::: "memory");
          v4u t[4], tm;
#pragma unroll
          for (int it = 0; it < 4; ++it) {
            const int off = ((8 * ck + it * 2 + (lane >> 5)) * 16 + mm * 4) * 128 + (lane & 31) * 16;
            t[it] = __builtin_amdgcn_raw_buffer_load_b128(rp, off, 0, EXLD_AUX);
          }
          const int offm = ((8 * ck + ((lane & 15) >> 1)) * 16 + mm * 4) * 8 + (lane & 1) * 16;
          tm = __builtin_amdgcn_raw_buffer_load_b128(rm, offm, 0, EXLD_AUX);
#pragma unroll
          for (int it = 0; it < 4; ++it) {
            const int off = ((8 * ck + it * 2 + (lane >> 5)) * 16 + mm * 4) * 128 + (lane & 31) * 16;
            *(LAS v4u*)(lds + L_P + off) = t[it];
          }
          if (lane < 16) *(LAS v4u*)(lds + L_MREF + offm) = tm;
        }
        waitlgkm();
        if (lane == 0) flg[4] = ck + 1;
        STAMP(1 + ck);
      }
    }
    bar();
    STAMP(3);
    const int hd = lane >> 5;
    f32x2 st[4], pa[4];
#pragma unroll
    for (int w = 0; w < 4; ++w) {
      st[w] = *(const LAS f32x2*)(lds + L_ST + w * 16 + hd * 8);
      pa[w] = *(const LAS f32x2*)(lds + L_MRG + w * 512 + lane * 8);
    }
    const float Mx = fmaxf(fmaxf(st[0].x, st[1].x), fmaxf(st[2].x, st[3].x));
    float Ls = 0.f, o0 = 0.f, o1 = 0.f;
#pragma unroll
    for (int w = 0; w < 4; ++w) {
      const float f = __builtin_amdgcn_exp2f(st[w].x - Mx);
      Ls = fmaf(f, st[w].y, Ls); o0 = fmaf(f, pa[w].x, o0); o1 = fmaf(f, pa[w].y, o1);
    }
    const float inv = 1.0f / Ls;
    const unsigned op = cvtpk(o0 * inv, o1 * inv);
    float y[8];
#pragma unroll
    for (int e = 0; e < 8; ++e) y[e] = 0.f;
#define STEP(i) { const unsigned ob = row_bcast<i>(op); _Pragma("unroll") for (int e = 0; e < 8; ++e) y[e] = dot2(wr[i][e], ob, y[e]); }
    STEP(0) STEP(1) STEP(2) STEP(3) STEP(4) STEP(5) STEP(6) STEP(7) STEP(8) STEP(9) STEP(10) STEP(11) STEP(12) STEP(13) STEP(14) STEP(15)
#undef STEP
    float z[4];
#pragma unroll
    for (int e = 0; e < 4; ++e) { auto s = __builtin_amdgcn_permlane32_swap(asu(y[e]), asu(y[e + 4]), false, false); z[e] = asf(s[0]) + asf(s[1]); }
    float u[2];
#pragma unroll
    for (int e = 0; e < 2; ++e) { auto s = __builtin_amdgcn_permlane16_swap(asu(z[e]), asu(z[e + 2]), false, false); u[e] = asf(s[0]) + asf(s[1]); }
    if (c < 14) {
      const int n0 = hw * 112 + c * 8 + g * 2;
      const __amdgpu_buffer_rsrc_t ri = RSRC(ws + W_INBOX + (size_t)b * (32 * 4096), 32 * 4096);
      __builtin_amdgcn_raw_buffer_store_b128(v4u{asu(u[0]), TAG, asu(u[1]), TAG}, ri, (n0 >> 4) * 4096 + jb * 128 + (n0 & 15) * 8, 0, SC1);
    }
    STAMP(4);
  }
  STAMP_FLUSH();
}

extern "C" void kernel_launch(void* const* d_in, const int* in_sizes, int n_in, void* d_out, int out_size, void* d_ws, size_t ws_size, hipStream_t stream) {
  (void)hipFuncSetAttribute((const void*)k_main, hipFuncAttributeMaxDynamicSharedMemorySize, L_END);
  k_q<<<256, 256, 0, stream>>>((const char*)d_in[0], (const char*)d_in[3], (char*)d_ws);
  k_main<<<256, 512, L_END, stream>>>((const char*)d_in[1], (const char*)d_in[2], (const char*)d_in[4], (char*)d_ws, (unsigned*)d_out);
}
```

```cpp
#include <hip/hip_runtime.h>
#include <hip/hip_bf16.h>
#include <type_traits>

typedef unsigned v4u __attribute__((ext_vector_type(4)));
typedef unsigned v2u __attribute__((ext_vector_type(2)));
typedef float f32x4 __attribute__((ext_vector_type(4)));
typedef float f32x2 __attribute__((ext_vector_type(2)));
typedef short bf16x8 __attribute__((ext_vector_type(8)));
typedef short s16x4 __attribute__((ext_vector_type(4)));
typedef __bf16 bf16x2 __attribute__((ext_vector_type(2)));
#define LAS __attribute__((address_space(3)))
#define GAS __attribute__((address_space(1)))

#define RS 4
#define NIF 5
#define NIFA NIF
#define NWI 4

constexpr int DM = 448, SK = 8192, DH = 256, NQ = 4096;
constexpr float C2 = 0.0625f * 1.4426950408889634f;
constexpr unsigned SPIN_CAP = 1u << 20;

constexpr size_t W_EPOCH = 0;
constexpr size_t W_Q = 4096;
constexpr size_t W_INB = W_Q + 65536;
constexpr size_t W_END = W_INB + 256 * 8192;

constexpr int L_RING = 0;
constexpr int L_SC = 4 * RS * 8192;
constexpr int L_Q = L_SC + 16384;
constexpr int L_ST = L_Q + 1024;
constexpr int L_TAG = L_ST + 64;
constexpr int L_END = L_TAG + 64;

template <int N> __device__ __forceinline__ void waitvm() { asm volatile("s_waitcnt vmcnt(%0)" ::"i"(N) : "memory"); }
__device__ __forceinline__ void waitlgkm() { asm volatile("s_waitcnt lgkmcnt(0)" ::: "memory"); }
__device__ __forceinline__ void bar() { asm volatile("s_waitcnt lgkmcnt(0)\n\ts_barrier" ::: "memory"); }
__device__ __forceinline__ void dma_nt(unsigned ldsaddr, unsigned voff, const void* sbase) {
  unsigned sv;
  asm volatile("s_mov_b32 %0, m0\n\ts_mov_b32 m0, %1\n\ts_nop 0\n\tglobal_load_lds_dwordx4 %2, %3 nt\n\ts_mov_b32 m0, %0" : "=&s"(sv) : "s"(ldsaddr), "v"(voff), "s"(sbase) : "memory");
}
__device__ __forceinline__ void dma_def(unsigned ldsaddr, unsigned voff, const void* sbase) {
  unsigned sv;
  asm volatile("s_mov_b32 %0, m0\n\ts_mov_b32 m0, %1\n\ts_nop 0\n\tglobal_load_lds_dwordx4 %2, %3\n\ts_mov_b32 m0, %0" : "=&s"(sv) : "s"(ldsaddr), "v"(voff), "s"(sbase) : "memory");
}
__device__ __forceinline__ f32x4 mfma32(v4u a, v4u b, f32x4 c) {
  return __builtin_amdgcn_mfma_f32_16x16x32_bf16(__builtin_bit_cast(bf16x8, a), __builtin_bit_cast(bf16x8, b), c, 0, 0, 0);
}
__device__ __forceinline__ f32x4 mfma16(v2u a, v2u b, f32x4 c) {
  return __builtin_amdgcn_mfma_f32_16x16x16bf16_1k(__builtin_bit_cast(s16x4, a), __builtin_bit_cast(s16x4, b), c, 0, 0, 0);
}
__device__ __forceinline__ v2u ldtr(const LAS char* p) {
  return __builtin_bit_cast(v2u, __builtin_amdgcn_ds_read_tr16_b64_v4i16((LAS s16x4*)p));
}
__device__ __forceinline__ unsigned cvtpk(float lo, float hi) { return __builtin_bit_cast(unsigned, __builtin_convertvector(f32x2{lo, hi}, bf16x2)); }
__device__ __forceinline__ float dot2(unsigned a, unsigned b, float acc) {
  return __builtin_amdgcn_fdot2_f32_bf16(__builtin_bit_cast(bf16x2, a), __builtin_bit_cast(bf16x2, b), acc, false);
}
__device__ __forceinline__ float asf(unsigned u) { return __builtin_bit_cast(float, u); }
__device__ __forceinline__ unsigned asu(float f) { return __builtin_bit_cast(unsigned, f); }
__device__ __forceinline__ float ex2(float x) { return __builtin_amdgcn_exp2f(x); }
__device__ __forceinline__ float rows_max(float x) {
  auto s = __builtin_amdgcn_permlane16_swap(asu(x), asu(x), false, false);
  x = fmaxf(asf(s[0]), asf(s[1]));
  auto t = __builtin_amdgcn_permlane32_swap(asu(x), asu(x), false, false);
  return fmaxf(asf(t[0]), asf(t[1]));
}
__device__ __forceinline__ float rows_sum(float x) {
  auto s = __builtin_amdgcn_permlane16_swap(asu(x), asu(x), false, false);
  x = asf(s[0]) + asf(s[1]);
  auto t = __builtin_amdgcn_permlane32_swap(asu(x), asu(x), false, false);
  return asf(t[0]) + asf(t[1]);
}
template <int N> __device__ __forceinline__ unsigned row_bcast(unsigned x) { return (unsigned)__builtin_amdgcn_mov_dpp((int)x, 0x150 + N, 0xf, 0xf, true); }
#define RSRC(p, n) __builtin_amdgcn_make_buffer_rsrc((void*)(p), 0, (int)(n), 0x00020000)
constexpr int SC1 = 16;
#define STAMP(i) do {} while (0)
#define STAMP_FLUSH() do {} while (0)

__global__ void __launch_bounds__(256) k_q(const char* __restrict__ x, const char* __restrict__ wq, const char* __restrict__ wo, char* __restrict__ ws) {
  __shared__ __attribute__((aligned(16))) char lds[14 * 1024 + 4096 + 16384];
  const int tid = threadIdx.x, lane = tid & 63, wave = __builtin_amdgcn_readfirstlane(tid >> 6);
  const int j = blockIdx.x, cb = (j & 7) * 32 + (j >> 3);
  const int g = lane >> 4, c = lane & 15;
  const unsigned l0 = (unsigned)(size_t)(LAS char*)lds;
  const unsigned voff = (lane >> 1) * 8192 + (lane & 1) * 16;
#pragma unroll
  for (int i = 0; i < 4; ++i) {
    const int s = wave + 4 * i;
    if (s < 14) dma_def(l0 + s * 1024, voff, wq + (size_t)s * 32 * 8192 + cb * 32);
  }
  v4u xf[4];
#pragma unroll
  for (int i = 0; i < 4; ++i) {
    const int s = (wave + 4 * i < 14) ? wave + 4 * i : 0;
    xf[i] = *(const GAS v4u*)(x + (c & 7) * (DM * 2) + s * 64 + g * 16);
  }
  *(GAS v4u*)(ws + W_INB + (size_t)j * 8192 + tid * 16) = v4u{0, 0, 0, 0};
  if (tid < 216) *(GAS v4u*)(ws + W_INB + (size_t)j * 8192 + 4096 + tid * 16) = v4u{0, 0, 0, 0};
  waitvm<0>();
  f32x4 acc = {0.f, 0.f, 0.f, 0.f};
  const LAS char* lp = (const LAS char*)lds + (8 * g + ((lane >> 2) & 3)) * 32 + (lane & 3) * 8;
#pragma unroll
  for (int i = 0; i < 4; ++i) {
    const int s = wave + 4 * i;
    if (s < 14) {
      const v2u a0 = ldtr(lp + s * 1024), a1 = ldtr(lp + s * 1024 + 128);
      acc = mfma32(v4u{a0.x, a0.y, a1.x, a1.y}, xf[i], acc);
    }
  }
  *(LAS f32x4*)((LAS char*)lds + 14 * 1024 + wave * 1024 + lane * 16) = acc;
  bar();
  if (wave == 0 && c < 8) {
    const LAS f32x4* r = (const LAS f32x4*)((LAS char*)lds + 14 * 1024 + lane * 16);
    const f32x4 s = (r[0] + r[64]) + (r[128] + r[192]);
    *(GAS v2u*)(ws + W_Q + (size_t)c * (NQ * 2) + (cb * 16 + 4 * g) * 2) = v2u{cvtpk(s[0], s[1]), cvtpk(s[2], s[3])};
  }
}

__global__ void __launch_bounds__(512, 2) k_main(const char* __restrict__ Kc, const char* __restrict__ Vc, const char* __restrict__ wo, char* __restrict__ ws, unsigned* __restrict__ out) {
  extern __shared__ __attribute__((aligned(16))) char lds_[];
  LAS char* lds = (LAS char*)lds_;
  const int tid = threadIdx.x, lane = tid & 63, wave = __builtin_amdgcn_readfirstlane(tid >> 6);
  const int bid = blockIdx.x, grp = bid >> 2, m = bid & 3, b = bid >> 5, jb = bid & 31, kvh = jb >> 2;
  const int g = lane >> 4, c = lane & 15, hd = lane >> 5;
  STAMP(0);
  auto merge4 = [&](int sl, float& Mx, float& Ls, float& o0, float& o1) {
    f32x2 st[4], pa[4];
#pragma unroll
    for (int w = 0; w < 4; ++w) {
      st[w] = *(const LAS f32x2*)(lds + L_ST + w * 16 + hd * 8);
      pa[w] = *(const LAS f32x2*)(lds + L_RING + w * (RS * 8192) + hd * 1024 + sl * 256 + (lane & 31) * 8);
    }
    Mx = fmaxf(fmaxf(st[0].x, st[1].x), fmaxf(st[2].x, st[3].x));
    Ls = 0.f; o0 = 0.f; o1 = 0.f;
#pragma unroll
    for (int w = 0; w < 4; ++w) {
      const float f = ex2(st[w].x - Mx);
      Ls = fmaf(f, st[w].y, Ls); o0 = fmaf(f, pa[w].x, o0); o1 = fmaf(f, pa[w].y, o1);
    }
  };
  if (wave < 4) {
    __builtin_amdgcn_s_setprio(3);
    const size_t t0 = (size_t)grp * (SK * DH * 2) + (NWI == 4 ? (size_t)m * 1048576 + wave * 8192 : (size_t)(m * 4 + wave) * 8192);
    const char* kb = Kc + t0;
    const char* vb = Vc + t0;
    const unsigned ring0 = (unsigned)(size_t)lds + L_RING + wave * (RS * 8192);
    const unsigned kvo = hd * 512 + (((lane & 31) ^ hd) * 16);
    const unsigned vvo = hd * 512 + (((lane & 31) ^ (hd << 1)) * 16);
    auto srcof = [&](int s) -> const char* { return (s < 32 ? kb : vb) + (size_t)(s & 31) * (NWI * 8192); };
#define ISS_K(p) do { waitvm<NIFA - 1>(); dma_nt(dst + (p) * 1024, kvo ^ ((p) << 5), src + (p) * 1024); } while (0)
#define ISS_V(p) do { waitvm<NIF - 1>(); dma_nt(dst + (p) * 1024, vvo ^ (((p) & 3) << 6), src + (p) * 1024); } while (0)
#define ISS(MODE, p) do { if (MODE == 0) ISS_K(p); else if (MODE == 1) ISS_V(p); } while (0)
    for (int s = 0; s < RS; ++s) {
      const unsigned dst = ring0 + s * 8192; const char* src = srcof(s);
#pragma unroll
      for (int p = 0; p < 8; ++p) ISS_K(p);
    }
    bar();
    STAMP(1);
    v4u qf[8];
#pragma unroll
    for (int s = 0; s < 8; ++s) qf[s] = *(const LAS v4u*)(lds + L_Q + (c & 1) * 512 + s * 64 + g * 16);
    const LAS char* wring = lds + L_RING + wave * (RS * 8192);
    LAS char* scw = lds + L_SC + wave * 4096 + (g * 2 + (c & 1)) * 16;
    const int kro = c * 512 + ((g ^ c) * 16);
    using M0_ = std::integral_constant<int, 0>; using M1_ = std::integral_constant<int, 1>; using M2_ = std::integral_constant<int, 2>;
    int fs = RS - 1, cs = 0;
    float mx = -INFINITY;
    auto qk = [&](auto mode, int s) {
      constexpr int MODE = decltype(mode)::value;
      const LAS char* base = wring + cs * 8192;
      const unsigned dst = ring0 + fs * 8192; const char* src = srcof(s + RS - 1);
      f32x4 a = {0.f, 0.f, 0.f, 0.f};
#pragma unroll
      for (int p = 0; p < 8; ++p) {
        ISS(MODE, p);
        const v4u kf = *(const LAS v4u*)(base + ((kro ^ ((p & 3) << 6)) + (p >> 2) * 256));
        a = mfma32(kf, qf[p], a);
        asm volatile("" : "+v"(a));
      }
      if (c < 2) *(LAS f32x4*)(scw + s * 128) = a;
      mx = fmaxf(fmaxf(mx, a[0]), fmaxf(fmaxf(a[1], a[2]), a[3]));
      fs = cs; cs = (cs == RS - 1) ? 0 : cs + 1;
    };
    waitvm<8 * (RS - 1)>();
    qk(M2_{}, 0);
    for (int s = 1; s < 33 - RS; ++s) { waitvm<8 * (RS - 2)>(); qk(M0_{}, s); }
    for (int s = 33 - RS; s < 32; ++s) { waitvm<8 * (RS - 2)>(); qk(M1_{}, s); }
    const float M = rows_max(mx) * C2;
    STAMP(2);
    f32x4 acc[16], accl = {0.f, 0.f, 0.f, 0.f};
#pragma unroll
    for (int d = 0; d < 16; ++d) acc[d] = f32x4{0.f, 0.f, 0.f, 0.f};
    const int kq = 4 * g + ((lane >> 2) & 3);
    const int vro = kq * 512 + (kq & 7) * 32 + (lane & 3) * 8;
    const v2u ones = {0x3F803F80u, 0x3F803F80u};
    auto pv = [&](auto mode, int i) {
      constexpr int MODE = decltype(mode)::value;
      const LAS char* base = wring + cs * 8192;
      const unsigned dst = ring0 + fs * 8192; const char* src = srcof(32 + i + RS - 1);
      const f32x4 sc = *(const LAS f32x4*)(scw + i * 128);
      const v2u pf = {cvtpk(ex2(fmaf(sc[0], C2, -M)), ex2(fmaf(sc[1], C2, -M))), cvtpk(ex2(fmaf(sc[2], C2, -M)), ex2(fmaf(sc[3], C2, -M)))};
      accl = mfma16(ones, pf, accl);
#pragma unroll
      for (int p = 0; p < 8; ++p) {
        ISS(MODE, p);
        const LAS char* a = base + (vro ^ (p << 5));
        acc[p] = mfma16(ldtr(a), pf, acc[p]);
        acc[p + 8] = mfma16(ldtr(a + 256), pf, acc[p + 8]);
        if (MODE != 2) asm volatile("" : "+v"(acc[p]), "+v"(acc[p + 8]));
      }
      fs = cs; cs = (cs == RS - 1) ? 0 : cs + 1;
    };
    for (int i = 0; i < 33 - RS; ++i) { waitvm<8 * (RS - 2)>(); pv(M1_{}, i); }
    for (int i = 33 - RS; i < 31; ++i) { waitvm<8>(); pv(M2_{}, i); }
    waitvm<0>();
    pv(M2_{}, 31);
    waitlgkm();
    if (c < 2) {
#pragma unroll
      for (int d = 0; d < 16; ++d) *(LAS f32x4*)(lds + L_RING + wave * (RS * 8192) + c * 1024 + (16 * d + 4 * g) * 4) = acc[d];
      if (g == 0) *(LAS f32x2*)(lds + L_ST + wave * 16 + c * 8) = f32x2{M, accl[0]};
    }
    const v2u tag = *(const LAS v2u*)(lds + L_TAG);
    STAMP(3);
    bar();
    if (wave != m) {
      float Mx, Ls, o0, o1;
      merge4(wave, Mx, Ls, o0, o1);
      const __amdgpu_buffer_rsrc_t ri = RSRC(ws + W_INB + (size_t)(grp * 4 + wave) * 8192 + 4096 + (((m - wave) & 3) - 1) * 1152, 1152);
      __builtin_amdgcn_raw_buffer_store_b128(v4u{asu(o0), tag.x, asu(o1), tag.y}, ri, lane * 16, 0, SC1);
      if ((lane & 31) == 0) __builtin_amdgcn_raw_buffer_store_b128(v4u{asu(Mx), tag.x, asu(Ls), tag.y}, ri, 1024 + hd * 16, 0, SC1);
      STAMP(4);
    } else if (jb < 28) {
      const __amdgpu_buffer_rsrc_t rs = RSRC(ws + W_INB + (size_t)bid * 8192, 4096);
      v4u v[4];
      for (unsigned sp = 0; sp < SPIN_CAP; ++sp) {
        asm volatile("" ::: "memory");
#pragma unroll
        for (int it = 0; it < 4; ++it) v[it] = __builtin_amdgcn_raw_buffer_load_b128(rs, it * 1024 + lane * 16, 0, SC1);
        bool ok = true;
#pragma unroll
        for (int it = 0; it < 4; ++it) ok &= (v[it].y == tag.x) & (v[it].w == tag.y);
        if (__all(ok)) break;
      }
      float s0 = (asf(v[0].x) + asf(v[1].x)) + (asf(v[2].x) + asf(v[3].x));
      float s1 = (asf(v[0].z) + asf(v[1].z)) + (asf(v[2].z) + asf(v[3].z));
      s0 += asf(__builtin_amdgcn_mov_dpp((int)asu(s0), 0x128, 0xf, 0xf, true));
      s1 += asf(__builtin_amdgcn_mov_dpp((int)asu(s1), 0x128, 0xf, 0xf, true));
      s0 = rows_sum(s0); s1 = rows_sum(s1);
      if (lane < 8) out[b * (DM / 2) + jb * 8 + lane] = cvtpk(s0, s1);
      STAMP(5);
    }
  } else {
    const int hw = wave - 4;
    if (hw == 0) {
      const v4u qv = *(const GAS v4u*)(ws + W_Q + (size_t)b * (NQ * 2) + kvh * 1024 + lane * 16);
      const v2u tg = {0x5A5A0001u, 0x5A5A0001u};
      *(LAS v4u*)(lds + L_Q + lane * 16) = qv;
      if (lane == 0) *(LAS v2u*)(lds + L_TAG) = tg;
    }
    bar();
    const int ch = c < 14 ? c : 13;
    const char* wp = wo + (size_t)((2 * kvh + (g >> 1)) * 256 + 64 * m + 32 * (g & 1)) * (DM * 2) + hw * 224 + ch * 16;
    unsigned wr[16][8];
#define WOB 2
#pragma unroll
    for (int i0 = 0; i0 < 16; i0 += WOB) {
      v4u r[2 * WOB];
#pragma unroll
      for (int u = 0; u < 2 * WOB; ++u) r[u] = *(const GAS v4u*)(wp + (size_t)(2 * i0 + u) * (DM * 2));
#pragma unroll
      for (int u = 0; u < WOB; ++u)
#pragma unroll
        for (int e = 0; e < 4; ++e) {
          wr[i0 + u][2 * e] = __builtin_amdgcn_perm(r[2 * u + 1][e], r[2 * u][e], 0x05040100u);
          wr[i0 + u][2 * e + 1] = __builtin_amdgcn_perm(r[2 * u + 1][e], r[2 * u][e], 0x07060302u);
        }
#pragma unroll
      for (int u = 0; u < WOB; ++u)
        asm volatile("" : "+v"(wr[i0 + u][0]), "+v"(wr[i0 + u][1]), "+v"(wr[i0 + u][2]), "+v"(wr[i0 + u][3]), "+v"(wr[i0 + u][4]), "+v"(wr[i0 + u][5]), "+v"(wr[i0 + u][6]), "+v"(wr[i0 + u][7]) :: "memory");
    }
    STAMP(1);
    const v2u tag = *(const LAS v2u*)(lds + L_TAG);
    bar();
    STAMP(3);
    float Mx, Ls, o0, o1;
    merge4(m, Mx, Ls, o0, o1);
    {
      const __amdgpu_buffer_rsrc_t rs = RSRC(ws + W_INB + (size_t)bid * 8192 + 4096, 3 * 1152);
      v4u v[3], s[3];
      for (unsigned sp = 0; sp < SPIN_CAP; ++sp) {
        asm volatile("" ::: "memory");
#pragma unroll
        for (int it = 0; it < 3; ++it) {
          v[it] = __builtin_amdgcn_raw_buffer_load_b128(rs, it * 1152 + lane * 16, 0, SC1);
          s[it] = __builtin_amdgcn_raw_buffer_load_b128(rs, it * 1152 + 1024 + hd * 16, 0, SC1);
        }
        bool ok = true;
#pragma unroll
        for (int it = 0; it < 3; ++it) ok &= (v[it].y == tag.x) & (v[it].w == tag.y) & (s[it].y == tag.x) & (s[it].w == tag.y);
        if (__all(ok)) break;
      }
      STAMP(4);
      const float Mg = fmaxf(fmaxf(Mx, asf(s[0].x)), fmaxf(asf(s[1].x), asf(s[2].x)));
      const float f = ex2(Mx - Mg);
      Ls *= f; o0 *= f; o1 *= f;
#pragma unroll
      for (int it = 0; it < 3; ++it) {
        const float fi = ex2(asf(s[it].x) - Mg);
        Ls = fmaf(fi, asf(s[it].z), Ls); o0 = fmaf(fi, asf(v[it].x), o0); o1 = fmaf(fi, asf(v[it].z), o1);
      }
    }
    const float inv = __builtin_amdgcn_rcpf(Ls);
    const unsigned op = cvtpk(o0 * inv, o1 * inv);
    float y[8];
#pragma unroll
    for (int e = 0; e < 8; ++e) y[e] = 0.f;
#define STEP(i) { const unsigned ob = row_bcast<i>(op); _Pragma("unroll") for (int e = 0; e < 8; ++e) y[e] = dot2(wr[i][e], ob, y[e]); }
    STEP(0) STEP(1) STEP(2) STEP(3) STEP(4) STEP(5) STEP(6) STEP(7) STEP(8) STEP(9) STEP(10) STEP(11) STEP(12) STEP(13) STEP(14) STEP(15)
#undef STEP
    float z[4];
#pragma unroll
    for (int e = 0; e < 4; ++e) { auto s = __builtin_amdgcn_permlane32_swap(asu(y[e]), asu(y[e + 4]), false, false); z[e] = asf(s[0]) + asf(s[1]); }
    float u[2];
#pragma unroll
    for (int e = 0; e < 2; ++e) { auto s = __builtin_amdgcn_permlane16_swap(asu(z[e]), asu(z[e + 2]), false, false); u[e] = asf(s[0]) + asf(s[1]); }
    if (c < 14) {
      const int n0 = hw * 112 + c * 8 + g * 2;
      const __amdgpu_buffer_rsrc_t ri = RSRC(ws + W_INB + (size_t)b * (32 * 8192), 32 * 8192);
      __builtin_amdgcn_raw_buffer_store_b128(v4u{asu(u[0]), tag.x, asu(u[1]), tag.y}, ri, (n0 >> 4) * 8192 + jb * 128 + (n0 & 15) * 8, 0, SC1);
    }
    STAMP(5);
  }
  STAMP_FLUSH();
}

extern "C" void kernel_launch(void* const* d_in, const int* in_sizes, int n_in, void* d_out, int out_size, void* d_ws, size_t ws_size, hipStream_t stream) {
  (void)hipFuncSetAttribute((const void*)k_main, hipFuncAttributeMaxDynamicSharedMemorySize, L_END);
  k_q<<<256, 256, 0, stream>>>((const char*)d_in[0], (const char*)d_in[3], (const char*)d_in[4], (char*)d_ws);
  k_main<<<256, 512, L_END, stream>>>((const char*)d_in[1], (const char*)d_in[2], (const char*)d_in[4], (char*)d_ws, (unsigned*)d_out);
}
```

```cpp
#define EPOCH
#define NIF 6
#include <hip/hip_runtime.h>
#include <hip/hip_bf16.h>
#include <type_traits>

typedef unsigned v4u __attribute__((ext_vector_type(4)));
typedef unsigned v2u __attribute__((ext_vector_type(2)));
typedef float f32x4 __attribute__((ext_vector_type(4)));
typedef float f32x2 __attribute__((ext_vector_type(2)));
typedef short bf16x8 __attribute__((ext_vector_type(8)));
typedef short s16x4 __attribute__((ext_vector_type(4)));
typedef __bf16 bf16x2 __attribute__((ext_vector_type(2)));
#define LAS __attribute__((address_space(3)))
#define GAS __attribute__((address_space(1)))

#define RS 4
#define NIFA NIF
#define NWI 4

constexpr int DM = 448, SK = 8192, DH = 256, NQ = 4096;
constexpr float C2 = 0.0625f * 1.4426950408889634f;
constexpr unsigned SPIN_CAP = 1u << 20;

constexpr size_t W_EPOCH = 0;
constexpr size_t W_Q = 4096;
constexpr size_t W_INB = W_Q + 65536;
constexpr size_t W_END = W_INB + 256 * 8192;

constexpr int L_RING = 0;
constexpr int L_SC = 4 * RS * 8192;
constexpr int L_Q = L_SC + 16384;
constexpr int L_ST = L_Q + 1024;
constexpr int L_TAG = L_ST + 64;
constexpr int L_END = L_TAG + 64;

template <int N> __device__ __forceinline__ void waitvm() { asm volatile("s_waitcnt vmcnt(%0)" ::"i"(N) : "memory"); }
__device__ __forceinline__ void waitlgkm() { asm volatile("s_waitcnt lgkmcnt(0)" ::: "memory"); }
__device__ __forceinline__ void bar() { asm volatile("s_waitcnt lgkmcnt(0)\n\ts_barrier" ::: "memory"); }
__device__ __forceinline__ void dma_nt(unsigned ldsaddr, unsigned voff, const void* sbase) {
  unsigned sv;
  asm volatile("s_mov_b32 %0, m0\n\ts_mov_b32 m0, %1\n\ts_nop 0\n\tglobal_load_lds_dwordx4 %2, %3 nt\n\ts_mov_b32 m0, %0" : "=&s"(sv) : "s"(ldsaddr), "v"(voff), "s"(sbase) : "memory");
}
__device__ __forceinline__ void dma_def(unsigned ldsaddr, unsigned voff, const void* sbase) {
  unsigned sv;
  asm volatile("s_mov_b32 %0, m0\n\ts_mov_b32 m0, %1\n\ts_nop 0\n\tglobal_load_lds_dwordx4 %2, %3\n\ts_mov_b32 m0, %0" : "=&s"(sv) : "s"(ldsaddr), "v"(voff), "s"(sbase) : "memory");
}
__device__ __forceinline__ f32x4 mfma32(v4u a, v4u b, f32x4 c) {
  return __builtin_amdgcn_mfma_f32_16x16x32_bf16(__builtin_bit_cast(bf16x8, a), __builtin_bit_cast(bf16x8, b), c, 0, 0, 0);
}
__device__ __forceinline__ f32x4 mfma16(v2u a, v2u b, f32x4 c) {
  return __builtin_amdgcn_mfma_f32_16x16x16bf16_1k(__builtin_bit_cast(s16x4, a), __builtin_bit_cast(s16x4, b), c, 0, 0, 0);
}
__device__ __forceinline__ v2u ldtr(const LAS char* p) {
  return __builtin_bit_cast(v2u, __builtin_amdgcn_ds_read_tr16_b64_v4i16((LAS s16x4*)p));
}
__device__ __forceinline__ unsigned cvtpk(float lo, float hi) { return __builtin_bit_cast(unsigned, __builtin_convertvector(f32x2{lo, hi}, bf16x2)); }
__device__ __forceinline__ float dot2(unsigned a, unsigned b, float acc) {
  return __builtin_amdgcn_fdot2_f32_bf16(__builtin_bit_cast(bf16x2, a), __builtin_bit_cast(bf16x2, b), acc, false);
}
__device__ __forceinline__ float asf(unsigned u) { return __builtin_bit_cast(float, u); }
__device__ __forceinline__ unsigned asu(float f) { return __builtin_bit_cast(unsigned, f); }
__device__ __forceinline__ float ex2(float x) { return __builtin_amdgcn_exp2f(x); }
__device__ __forceinline__ float rows_max(float x) {
  auto s = __builtin_amdgcn_permlane16_swap(asu(x), asu(x), false, false);
  x = fmaxf(asf(s[0]), asf(s[1]));
  auto t = __builtin_amdgcn_permlane32_swap(asu(x), asu(x), false, false);
  return fmaxf(asf(t[0]), asf(t[1]));
}
__device__ __forceinline__ float rows_sum(float x) {
  auto s = __builtin_amdgcn_permlane16_swap(asu(x), asu(x), false, false);
  x = asf(s[0]) + asf(s[1]);
  auto t = __builtin_amdgcn_permlane32_swap(asu(x), asu(x), false, false);
  return asf(t[0]) + asf(t[1]);
}
template <int N> __device__ __forceinline__ unsigned row_bcast(unsigned x) { return (unsigned)__builtin_amdgcn_mov_dpp((int)x, 0x150 + N, 0xf, 0xf, true); }
#define RSRC(p, n) __builtin_amdgcn_make_buffer_rsrc((void*)(p), 0, (int)(n), 0x00020000)
constexpr int SC1 = 16;
#define STAMP(i) do {} while (0)
#define STAMP_FLUSH() do {} while (0)

__global__ void __launch_bounds__(256) k_q(const char* __restrict__ x, const char* __restrict__ wq, const char* __restrict__ wo, char* __restrict__ ws) {
  __shared__ __attribute__((aligned(16))) char lds[14 * 1024 + 4096 + 16384];
  const int tid = threadIdx.x, lane = tid & 63, wave = __builtin_amdgcn_readfirstlane(tid >> 6);
  const int j = blockIdx.x, cb = (j & 7) * 32 + (j >> 3);
  const int g = lane >> 4, c = lane & 15;
  const unsigned l0 = (unsigned)(size_t)(LAS char*)lds;
  const unsigned voff = (lane >> 1) * 8192 + (lane & 1) * 16;
#pragma unroll
  for (int i = 0; i < 4; ++i) {
    const int s = wave + 4 * i;
    if (s < 14) dma_def(l0 + s * 1024, voff, wq + (size_t)s * 32 * 8192 + cb * 32);
  }
  v4u xf[4];
#pragma unroll
  for (int i = 0; i < 4; ++i) {
    const int s = (wave + 4 * i < 14) ? wave + 4 * i : 0;
    xf[i] = *(const GAS v4u*)(x + (c & 7) * (DM * 2) + s * 64 + g * 16);
  }
  if (j == 0 && tid == 0) { GAS unsigned long long* e = (GAS unsigned long long*)(ws + W_EPOCH); *e = *e + 0x9E3779B97F4A7C15ull; }
  waitvm<0>();
  f32x4 acc = {0.f, 0.f, 0.f, 0.f};
  const LAS char* lp = (const LAS char*)lds + (8 * g + ((lane >> 2) & 3)) * 32 + (lane & 3) * 8;
#pragma unroll
  for (int i = 0; i < 4; ++i) {
    const int s = wave + 4 * i;
    if (s < 14) {
      const v2u a0 = ldtr(lp + s * 1024), a1 = ldtr(lp + s * 1024 + 128);
      acc = mfma32(v4u{a0.x, a0.y, a1.x, a1.y}, xf[i], acc);
    }
  }
  *(LAS f32x4*)((LAS char*)lds + 14 * 1024 + wave * 1024 + lane * 16) = acc;
  bar();
  if (wave == 0 && c < 8) {
    const LAS f32x4* r = (const LAS f32x4*)((LAS char*)lds + 14 * 1024 + lane * 16);
    const f32x4 s = (r[0] + r[64]) + (r[128] + r[192]);
    *(GAS v2u*)(ws + W_Q + (size_t)c * (NQ * 2) + (cb * 16 + 4 * g) * 2) = v2u{cvtpk(s[0], s[1]), cvtpk(s[2], s[3])};
  }
}

__global__ void __launch_bounds__(512, 2) k_main(const char* __restrict__ Kc, const char* __restrict__ Vc, const char* __restrict__ wo, char* __restrict__ ws, unsigned* __restrict__ out) {
  extern __shared__ __attribute__((aligned(16))) char lds_[];
  LAS char* lds = (LAS char*)lds_;
  const int tid = threadIdx.x, lane = tid & 63, wave = __builtin_amdgcn_readfirstlane(tid >> 6);
  const int bid = blockIdx.x, grp = bid >> 2, m = bid & 3, b = bid >> 5, jb = bid & 31, kvh = jb >> 2;
  const int g = lane >> 4, c = lane & 15, hd = lane >> 5;
  STAMP(0);
  auto merge4 = [&](int sl, float& Mx, float& Ls, float& o0, float& o1) {
    f32x2 st[4], pa[4];
#pragma unroll
    for (int w = 0; w < 4; ++w) {
      st[w] = *(const LAS f32x2*)(lds + L_ST + w * 16 + hd * 8);
      pa[w] = *(const LAS f32x2*)(lds + L_RING + w * (RS * 8192) + hd * 1024 + sl * 256 + (lane & 31) * 8);
    }
    Mx = fmaxf(fmaxf(st[0].x, st[1].x), fmaxf(st[2].x, st[3].x));
    Ls = 0.f; o0 = 0.f; o1 = 0.f;
#pragma unroll
    for (int w = 0; w < 4; ++w) {
      const float f = ex2(st[w].x - Mx);
      Ls = fmaf(f, st[w].y, Ls); o0 = fmaf(f, pa[w].x, o0); o1 = fmaf(f, pa[w].y, o1);
    }
  };
  if (wave < 4) {
    __builtin_amdgcn_s_setprio(3);
    const size_t t0 = (size_t)grp * (SK * DH * 2) + (NWI == 4 ? (size_t)m * 1048576 + wave * 8192 : (size_t)(m * 4 + wave) * 8192);
    const char* kb = Kc + t0;
    const char* vb = Vc + t0;
    const unsigned ring0 = (unsigned)(size_t)lds + L_RING + wave * (RS * 8192);
    const unsigned kvo = hd * 512 + (((lane & 31) ^ hd) * 16);
    const unsigned vvo = hd * 512 + (((lane & 31) ^ (hd << 1)) * 16);
    auto srcof = [&](int s) -> const char* { return (s < 32 ? kb : vb) + (size_t)(s & 31) * (NWI * 8192); };
#define ISS_K(p) do { waitvm<NIFA - 1>(); dma_nt(dst + (p) * 1024, kvo ^ ((p) << 5), src + (p) * 1024); } while (0)
#define ISS_V(p) do { waitvm<NIF - 1>(); dma_nt(dst + (p) * 1024, vvo ^ (((p) & 3) << 6), src + (p) * 1024); } while (0)
#define ISS(MODE, p) do { if (MODE == 0) ISS_K(p); else if (MODE == 1) ISS_V(p); } while (0)
    for (int s = 0; s < RS; ++s) {
      const unsigned dst = ring0 + s * 8192; const char* src = srcof(s);
#pragma unroll
      for (int p = 0; p < 8; ++p) ISS_K(p);
    }
    bar();
    STAMP(1);
    v4u qf[8];
#pragma unroll
    for (int s = 0; s < 8; ++s) qf[s] = *(const LAS v4u*)(lds + L_Q + (c & 1) * 512 + s * 64 + g * 16);
    const LAS char* wring = lds + L_RING + wave * (RS * 8192);
    LAS char* scw = lds + L_SC + wave * 4096 + (g * 2 + (c & 1)) * 16;
    const int kro = c * 512 + ((g ^ c) * 16);
    using M0_ = std::integral_constant<int, 0>; using M1_ = std::integral_constant<int, 1>; using M2_ = std::integral_constant<int, 2>;
    int fs = RS - 1, cs = 0;
    float mx = -INFINITY;
    auto qk = [&](auto mode, int s) {
      constexpr int MODE = decltype(mode)::value;
      const LAS char* base = wring + cs * 8192;
      const unsigned dst = ring0 + fs * 8192; const char* src = srcof(s + RS - 1);
      f32x4 a = {0.f, 0.f, 0.f, 0.f};
#pragma unroll
      for (int p = 0; p < 8; ++p) {
        ISS(MODE, p);
        const v4u kf = *(const LAS v4u*)(base + ((kro ^ ((p & 3) << 6)) + (p >> 2) * 256));
        a = mfma32(kf, qf[p], a);
        asm volatile("" : "+v"(a));
      }
      if (c < 2) *(LAS f32x4*)(scw + s * 128) = a;
      mx = fmaxf(fmaxf(mx, a[0]), fmaxf(fmaxf(a[1], a[2]), a[3]));
      fs = cs; cs = (cs == RS - 1) ? 0 : cs + 1;
    };
    waitvm<8 * (RS - 1)>();
    qk(M2_{}, 0);
    for (int s = 1; s < 33 - RS; ++s) { waitvm<8 * (RS - 2)>(); qk(M0_{}, s); }
    for (int s = 33 - RS; s < 32; ++s) { waitvm<8 * (RS - 2)>(); qk(M1_{}, s); }
    const float M = rows_max(mx) * C2;
    STAMP(2);
    f32x4 acc[16], accl = {0.f, 0.f, 0.f, 0.f};
#pragma unroll
    for (int d = 0; d < 16; ++d) acc[d] = f32x4{0.f, 0.f, 0.f, 0.f};
    const int kq = 4 * g + ((lane >> 2) & 3);
    const int vro = kq * 512 + (kq & 7) * 32 + (lane & 3) * 8;
    const v2u ones = {0x3F803F80u, 0x3F803F80u};
    auto pv = [&](auto mode, int i) {
      constexpr int MODE = decltype(mode)::value;
      const LAS char* base = wring + cs * 8192;
      const unsigned dst = ring0 + fs * 8192; const char* src = srcof(32 + i + RS - 1);
      const f32x4 sc = *(const LAS f32x4*)(scw + i * 128);
      const v2u pf = {cvtpk(ex2(fmaf(sc[0], C2, -M)), ex2(fmaf(sc[1], C2, -M))), cvtpk(ex2(fmaf(sc[2], C2, -M)), ex2(fmaf(sc[3], C2, -M)))};
      accl = mfma16(ones, pf, accl);
#pragma unroll
      for (int p = 0; p < 8; ++p) {
        ISS(MODE, p);
        const LAS char* a = base + (vro ^ (p << 5));
        acc[p] = mfma16(ldtr(a), pf, acc[p]);
        acc[p + 8] = mfma16(ldtr(a + 256), pf, acc[p + 8]);
        if (MODE != 2) asm volatile("" : "+v"(acc[p]), "+v"(acc[p + 8]));
      }
      fs = cs; cs = (cs == RS - 1) ? 0 : cs + 1;
    };
    for (int i = 0; i < 33 - RS; ++i) { waitvm<8 * (RS - 2)>(); pv(M1_{}, i); }
    for (int i = 33 - RS; i < 31; ++i) { waitvm<8>(); pv(M2_{}, i); }
    waitvm<0>();
    pv(M2_{}, 31);
    waitlgkm();
    if (c < 2) {
#pragma unroll
      for (int d = 0; d < 16; ++d) *(LAS f32x4*)(lds + L_RING + wave * (RS * 8192) + c * 1024 + (16 * d + 4 * g) * 4) = acc[d];
      if (g == 0) *(LAS f32x2*)(lds + L_ST + wave * 16 + c * 8) = f32x2{M, accl[0]};
    }
    const v2u tag = *(const LAS v2u*)(lds + L_TAG);
    STAMP(3);
    bar();
    if (wave != m) {
      float Mx, Ls, o0, o1;
      merge4(wave, Mx, Ls, o0, o1);
      const __amdgpu_buffer_rsrc_t ri = RSRC(ws + W_INB + (size_t)(grp * 4 + wave) * 8192 + 4096 + (((m - wave) & 3) - 1) * 1152, 1152);
      __builtin_amdgcn_raw_buffer_store_b128(v4u{asu(o0), tag.x, asu(o1), tag.y}, ri, lane * 16, 0, SC1);
      if ((lane & 31) == 0) __builtin_amdgcn_raw_buffer_store_b128(v4u{asu(Mx), tag.x, asu(Ls), tag.y}, ri, 1024 + hd * 16, 0, SC1);
      STAMP(4);
    } else if (jb < 28) {
      const __amdgpu_buffer_rsrc_t rs = RSRC(ws + W_INB + (size_t)bid * 8192, 4096);
      v4u v[4];
      for (unsigned sp = 0; sp < SPIN_CAP; ++sp) {
        asm volatile("" ::: "memory");
#pragma unroll
        for (int it = 0; it < 4; ++it) v[it] = __builtin_amdgcn_raw_buffer_load_b128(rs, it * 1024 + lane * 16, 0, SC1);
        bool ok = true;
#pragma unroll
        for (int it = 0; it < 4; ++it) ok &= (v[it].y == tag.x) & (v[it].w == tag.y);
        if (__all(ok)) break;
      }
      float s0 = (asf(v[0].x) + asf(v[1].x)) + (asf(v[2].x) + asf(v[3].x));
      float s1 = (asf(v[0].z) + asf(v[1].z)) + (asf(v[2].z) + asf(v[3].z));
      s0 += asf(__builtin_amdgcn_mov_dpp((int)asu(s0), 0x128, 0xf, 0xf, true));
      s1 += asf(__builtin_amdgcn_mov_dpp((int)asu(s1), 0x128, 0xf, 0xf, true));
      s0 = rows_sum(s0); s1 = rows_sum(s1);
      if (lane < 8) out[b * (DM / 2) + jb * 8 + lane] = cvtpk(s0, s1);
      STAMP(5);
    }
  } else {
    const int hw = wave - 4;
    if (hw == 0) {
      const v4u qv = *(const GAS v4u*)(ws + W_Q + (size_t)b * (NQ * 2) + kvh * 1024 + lane * 16);
      const v2u tg = *(const GAS v2u*)(ws + W_EPOCH);
      *(LAS v4u*)(lds + L_Q + lane * 16) = qv;
      if (lane == 0) *(LAS v2u*)(lds + L_TAG) = tg;
    }
    bar();
    const int ch = c < 14 ? c : 13;
    const char* wp = wo + (size_t)((2 * kvh + (g >> 1)) * 256 + 64 * m + 32 * (g & 1)) * (DM * 2) + hw * 224 + ch * 16;
    unsigned wr[16][8];
#define WOB 2
#pragma unroll
    for (int i0 = 0; i0 < 16; i0 += WOB) {
      v4u r[2 * WOB];
#pragma unroll
      for (int u = 0; u < 2 * WOB; ++u) r[u] = *(const GAS v4u*)(wp + (size_t)(2 * i0 + u) * (DM * 2));
#pragma unroll
      for (int u = 0; u < WOB; ++u)
#pragma unroll
        for (int e = 0; e < 4; ++e) {
          wr[i0 + u][2 * e] = __builtin_amdgcn_perm(r[2 * u + 1][e], r[2 * u][e], 0x05040100u);
          wr[i0 + u][2 * e + 1] = __builtin_amdgcn_perm(r[2 * u + 1][e], r[2 * u][e], 0x07060302u);
        }
#pragma unroll
      for (int u = 0; u < WOB; ++u)
        asm volatile("" : "+v"(wr[i0 + u][0]), "+v"(wr[i0 + u][1]), "+v"(wr[i0 + u][2]), "+v"(wr[i0 + u][3]), "+v"(wr[i0 + u][4]), "+v"(wr[i0 + u][5]), "+v"(wr[i0 + u][6]), "+v"(wr[i0 + u][7]) :: "memory");
    }
    STAMP(1);
    const v2u tag = *(const LAS v2u*)(lds + L_TAG);
    bar();
    STAMP(3);
    float Mx, Ls, o0, o1;
    merge4(m, Mx, Ls, o0, o1);
    {
      const __amdgpu_buffer_rsrc_t rs = RSRC(ws + W_INB + (size_t)bid * 8192 + 4096, 3 * 1152);
      v4u v[3], s[3];
      for (unsigned sp = 0; sp < SPIN_CAP; ++sp) {
        asm volatile("" ::: "memory");
#pragma unroll
        for (int it = 0; it < 3; ++it) {
          v[it] = __builtin_amdgcn_raw_buffer_load_b128(rs, it * 1152 + lane * 16, 0, SC1);
          s[it] = __builtin_amdgcn_raw_buffer_load_b128(rs, it * 1152 + 1024 + hd * 16, 0, SC1);
        }
        bool ok = true;
#pragma unroll
        for (int it = 0; it < 3; ++it) ok &= (v[it].y == tag.x) & (v[it].w == tag.y) & (s[it].y == tag.x) & (s[it].w == tag.y);
        if (__all(ok)) break;
      }
      STAMP(4);
      const float Mg = fmaxf(fmaxf(Mx, asf(s[0].x)), fmaxf(asf(s[1].x), asf(s[2].x)));
      const float f = ex2(Mx - Mg);
      Ls *= f; o0 *= f; o1 *= f;
#pragma unroll
      for (int it = 0; it < 3; ++it) {
        const float fi = ex2(asf(s[it].x) - Mg);
        Ls = fmaf(fi, asf(s[it].z), Ls); o0 = fmaf(fi, asf(v[it].x), o0); o1 = fmaf(fi, asf(v[it].z), o1);
      }
    }
    const float inv = __builtin_amdgcn_rcpf(Ls);
    const unsigned op = cvtpk(o0 * inv, o1 * inv);
    float y[8];
#pragma unroll
    for (int e = 0; e < 8; ++e) y[e] = 0.f;
#define STEP(i) { const unsigned ob = row_bcast<i>(op); _Pragma("unroll") for (int e = 0; e < 8; ++e) y[e] = dot2(wr[i][e], ob, y[e]); }
    STEP(0) STEP(1) STEP(2) STEP(3) STEP(4) STEP(5) STEP(6) STEP(7) STEP(8) STEP(9) STEP(10) STEP(11) STEP(12) STEP(13) STEP(14) STEP(15)
#undef STEP
    float z[4];
#pragma unroll
    for (int e = 0; e < 4; ++e) { auto s = __builtin_amdgcn_permlane32_swap(asu(y[e]), asu(y[e + 4]), false, false); z[e] = asf(s[0]) + asf(s[1]); }
    float u[2];
#pragma unroll
    for (int e = 0; e < 2; ++e) { auto s = __builtin_amdgcn_permlane16_swap(asu(z[e]), asu(z[e + 2]), false, false); u[e] = asf(s[0]) + asf(s[1]); }
    if (c < 14) {
      const int n0 = hw * 112 + c * 8 + g * 2;
      const __amdgpu_buffer_rsrc_t ri = RSRC(ws + W_INB + (size_t)b * (32 * 8192), 32 * 8192);
      __builtin_amdgcn_raw_buffer_store_b128(v4u{asu(u[0]), tag.x, asu(u[1]), tag.y}, ri, (n0 >> 4) * 8192 + jb * 128 + (n0 & 15) * 8, 0, SC1);
    }
    STAMP(5);
  }
  STAMP_FLUSH();
}

extern "C" void kernel_launch(void* const* d_in, const int* in_sizes, int n_in, void* d_out, int out_size, void* d_ws, size_t ws_size, hipStream_t stream) {
  (void)hipFuncSetAttribute((const void*)k_main, hipFuncAttributeMaxDynamicSharedMemorySize, L_END);
  k_q<<<256, 256, 0, stream>>>((const char*)d_in[0], (const char*)d_in[3], (const char*)d_in[4], (char*)d_ws);
  k_main<<<256, 512, L_END, stream>>>((const char*)d_in[1], (const char*)d_in[2], (const char*)d_in[4], (char*)d_ws, (unsigned*)d_out);
}
```

```cpp
#define EPOCH
#define STAMPS
#include <cstdio>
#include <hip/hip_runtime.h>
#include <hip/hip_bf16.h>
#include <type_traits>

typedef unsigned v4u __attribute__((ext_vector_type(4)));
typedef unsigned v2u __attribute__((ext_vector_type(2)));
typedef float f32x4 __attribute__((ext_vector_type(4)));
typedef float f32x2 __attribute__((ext_vector_type(2)));
typedef short bf16x8 __attribute__((ext_vector_type(8)));
typedef short s16x4 __attribute__((ext_vector_type(4)));
typedef __bf16 bf16x2 __attribute__((ext_vector_type(2)));
#define LAS __attribute__((address_space(3)))
#define GAS __attribute__((address_space(1)))

#define RS 4
#define NIF 5
#define NIFA NIF
#define NWI 4

constexpr int DM = 448, SK = 8192, DH = 256, NQ = 4096;
constexpr float C2 = 0.0625f * 1.4426950408889634f;
constexpr unsigned SPIN_CAP = 1u << 20;

constexpr size_t W_EPOCH = 0;
constexpr size_t W_Q = 4096;
constexpr size_t W_INB = W_Q + 65536;
constexpr size_t W_END = W_INB + 256 * 8192;
constexpr size_t W_TS = 8u << 20;

constexpr int L_RING = 0;
constexpr int L_SC = 4 * RS * 8192;
constexpr int L_Q = L_SC + 16384;
constexpr int L_ST = L_Q + 1024;
constexpr int L_TAG = L_ST + 64;
constexpr int L_END = L_TAG + 64;

template <int N> __device__ __forceinline__ void waitvm() { asm volatile("s_waitcnt vmcnt(%0)" ::"i"(N) : "memory"); }
__device__ __forceinline__ void waitlgkm() { asm volatile("s_waitcnt lgkmcnt(0)" ::: "memory"); }
__device__ __forceinline__ void bar() { asm volatile("s_waitcnt lgkmcnt(0)\n\ts_barrier" ::: "memory"); }
__device__ __forceinline__ void dma_nt(unsigned ldsaddr, unsigned voff, const void* sbase) {
  unsigned sv;
  asm volatile("s_mov_b32 %0, m0\n\ts_mov_b32 m0, %1\n\ts_nop 0\n\tglobal_load_lds_dwordx4 %2, %3 nt\n\ts_mov_b32 m0, %0" : "=&s"(sv) : "s"(ldsaddr), "v"(voff), "s"(sbase) : "memory");
}
__device__ __forceinline__ void dma_def(unsigned ldsaddr, unsigned voff, const void* sbase) {
  unsigned sv;
  asm volatile("s_mov_b32 %0, m0\n\ts_mov_b32 m0, %1\n\ts_nop 0\n\tglobal_load_lds_dwordx4 %2, %3\n\ts_mov_b32 m0, %0" : "=&s"(sv) : "s"(ldsaddr), "v"(voff), "s"(sbase) : "memory");
}
__device__ __forceinline__ f32x4 mfma32(v4u a, v4u b, f32x4 c) {
  return __builtin_amdgcn_mfma_f32_16x16x32_bf16(__builtin_bit_cast(bf16x8, a), __builtin_bit_cast(bf16x8, b), c, 0, 0, 0);
}
__device__ __forceinline__ f32x4 mfma16(v2u a, v2u b, f32x4 c) {
  return __builtin_amdgcn_mfma_f32_16x16x16bf16_1k(__builtin_bit_cast(s16x4, a), __builtin_bit_cast(s16x4, b), c, 0, 0, 0);
}
__device__ __forceinline__ v2u ldtr(const LAS char* p) {
  return __builtin_bit_cast(v2u, __builtin_amdgcn_ds_read_tr16_b64_v4i16((LAS s16x4*)p));
}
__device__ __forceinline__ unsigned cvtpk(float lo, float hi) { return __builtin_bit_cast(unsigned, __builtin_convertvector(f32x2{lo, hi}, bf16x2)); }
__device__ __forceinline__ float dot2(unsigned a, unsigned b, float acc) {
  return __builtin_amdgcn_fdot2_f32_bf16(__builtin_bit_cast(bf16x2, a), __builtin_bit_cast(bf16x2, b), acc, false);
}
__device__ __forceinline__ float asf(unsigned u) { return __builtin_bit_cast(float, u); }
__device__ __forceinline__ unsigned asu(float f) { return __builtin_bit_cast(unsigned, f); }
__device__ __forceinline__ float ex2(float x) { return __builtin_amdgcn_exp2f(x); }
__device__ __forceinline__ float rows_max(float x) {
  auto s = __builtin_amdgcn_permlane16_swap(asu(x), asu(x), false, false);
  x = fmaxf(asf(s[0]), asf(s[1]));
  auto t = __builtin_amdgcn_permlane32_swap(asu(x), asu(x), false, false);
  return fmaxf(asf(t[0]), asf(t[1]));
}
__device__ __forceinline__ float rows_sum(float x) {
  auto s = __builtin_amdgcn_permlane16_swap(asu(x), asu(x), false, false);
  x = asf(s[0]) + asf(s[1]);
  auto t = __builtin_amdgcn_permlane32_swap(asu(x), asu(x), false, false);
  return asf(t[0]) + asf(t[1]);
}
template <int N> __device__ __forceinline__ unsigned row_bcast(unsigned x) { return (unsigned)__builtin_amdgcn_mov_dpp((int)x, 0x150 + N, 0xf, 0xf, true); }
#define RSRC(p, n) __builtin_amdgcn_make_buffer_rsrc((void*)(p), 0, (int)(n), 0x00020000)
constexpr int SC1 = 16;
#define STAMP(i) do { tsv[i] = __builtin_amdgcn_s_memrealtime(); asm volatile("s_waitcnt lgkmcnt(0)" : "+s"(tsv[i]) :: "memory"); } while (0)
#define STAMP_FLUSH() do { if (lane == 0) { _Pragma("unroll") for (int i_ = 0; i_ < 8; ++i_) ((unsigned long long*)(ws + W_TS))[(blockIdx.x * 8 + wave) * 8 + i_] = tsv[i_]; } } while (0)

__global__ void __launch_bounds__(256) k_q(const char* __restrict__ x, const char* __restrict__ wq, const char* __restrict__ wo, char* __restrict__ ws) {
  __shared__ __attribute__((aligned(16))) char lds[14 * 1024 + 4096 + 16384];
  const int tid = threadIdx.x, lane = tid & 63, wave = __builtin_amdgcn_readfirstlane(tid >> 6);
  const int j = blockIdx.x, cb = (j & 7) * 32 + (j >> 3);
  const int g = lane >> 4, c = lane & 15;
  const unsigned l0 = (unsigned)(size_t)(LAS char*)lds;
  const unsigned voff = (lane >> 1) * 8192 + (lane & 1) * 16;
#pragma unroll
  for (int i = 0; i < 4; ++i) {
    const int s = wave + 4 * i;
    if (s < 14) dma_def(l0 + s * 1024, voff, wq + (size_t)s * 32 * 8192 + cb * 32);
  }
  v4u xf[4];
#pragma unroll
  for (int i = 0; i < 4; ++i) {
    const int s = (wave + 4 * i < 14) ? wave + 4 * i : 0;
    xf[i] = *(const GAS v4u*)(x + (c & 7) * (DM * 2) + s * 64 + g * 16);
  }
  if (j == 0 && tid == 0) { GAS unsigned long long* e = (GAS unsigned long long*)(ws + W_EPOCH); *e = *e + 0x9E3779B97F4A7C15ull; }
  waitvm<0>();
  f32x4 acc = {0.f, 0.f, 0.f, 0.f};
  const LAS char* lp = (const LAS char*)lds + (8 * g + ((lane >> 2) & 3)) * 32 + (lane & 3) * 8;
#pragma unroll
  for (int i = 0; i < 4; ++i) {
    const int s = wave + 4 * i;
    if (s < 14) {
      const v2u a0 = ldtr(lp + s * 1024), a1 = ldtr(lp + s * 1024 + 128);
      acc = mfma32(v4u{a0.x, a0.y, a1.x, a1.y}, xf[i], acc);
    }
  }
  *(LAS f32x4*)((LAS char*)lds + 14 * 1024 + wave * 1024 + lane * 16) = acc;
  bar();
  if (wave == 0 && c < 8) {
    const LAS f32x4* r = (const LAS f32x4*)((LAS char*)lds + 14 * 1024 + lane * 16);
    const f32x4 s = (r[0] + r[64]) + (r[128] + r[192]);
    *(GAS v2u*)(ws + W_Q + (size_t)c * (NQ * 2) + (cb * 16 + 4 * g) * 2) = v2u{cvtpk(s[0], s[1]), cvtpk(s[2], s[3])};
  }
}

__global__ void __launch_bounds__(512, 2) k_main(const char* __restrict__ Kc, const char* __restrict__ Vc, const char* __restrict__ wo, char* __restrict__ ws, unsigned* __restrict__ out) {
  extern __shared__ __attribute__((aligned(16))) char lds_[];
  LAS char* lds = (LAS char*)lds_;
  const int tid = threadIdx.x, lane = tid & 63, wave = __builtin_amdgcn_readfirstlane(tid >> 6);
  const int bid = blockIdx.x, grp = bid >> 2, m = bid & 3, b = bid >> 5, jb = bid & 31, kvh = jb >> 2;
  const int g = lane >> 4, c = lane & 15, hd = lane >> 5;
  unsigned long long tsv[8] = {0, 0, 0, 0, 0, 0, 0, 0};
  STAMP(0);
  auto merge4 = [&](int sl, float& Mx, float& Ls, float& o0, float& o1) {
    f32x2 st[4], pa[4];
#pragma unroll
    for (int w = 0; w < 4; ++w) {
      st[w] = *(const LAS f32x2*)(lds + L_ST + w * 16 + hd * 8);
      pa[w] = *(const LAS f32x2*)(lds + L_RING + w * (RS * 8192) + hd * 1024 + sl * 256 + (lane & 31) * 8);
    }
    Mx = fmaxf(fmaxf(st[0].x, st[1].x), fmaxf(st[2].x, st[3].x));
    Ls = 0.f; o0 = 0.f; o1 = 0.f;
#pragma unroll
    for (int w = 0; w < 4; ++w) {
      const float f = ex2(st[w].x - Mx);
      Ls = fmaf(f, st[w].y, Ls); o0 = fmaf(f, pa[w].x, o0); o1 = fmaf(f, pa[w].y, o1);
    }
  };
  if (wave < 4) {
    __builtin_amdgcn_s_setprio(3);
    const size_t t0 = (size_t)grp * (SK * DH * 2) + (NWI == 4 ? (size_t)m * 1048576 + wave * 8192 : (size_t)(m * 4 + wave) * 8192);
    const char* kb = Kc + t0;
    const char* vb = Vc + t0;
    const unsigned ring0 = (unsigned)(size_t)lds + L_RING + wave * (RS * 8192);
    const unsigned kvo = hd * 512 + (((lane & 31) ^ hd) * 16);
    const unsigned vvo = hd * 512 + (((lane & 31) ^ (hd << 1)) * 16);
    auto srcof = [&](int s) -> const char* { return (s < 32 ? kb : vb) + (size_t)(s & 31) * (NWI * 8192); };
#define ISS_K(p) do { waitvm<NIFA - 1>(); dma_nt(dst + (p) * 1024, kvo ^ ((p) << 5), src + (p) * 1024); } while (0)
#define ISS_V(p) do { waitvm<NIF - 1>(); dma_nt(dst + (p) * 1024, vvo ^ (((p) & 3) << 6), src + (p) * 1024); } while (0)
#define ISS(MODE, p) do { if (MODE == 0) ISS_K(p); else if (MODE == 1) ISS_V(p); } while (0)
    for (int s = 0; s < RS; ++s) {
      const unsigned dst = ring0 + s * 8192; const char* src = srcof(s);
#pragma unroll
      for (int p = 0; p < 8; ++p) ISS_K(p);
    }
    bar();
    STAMP(1);
    v4u qf[8];
#pragma unroll
    for (int s = 0; s < 8; ++s) qf[s] = *(const LAS v4u*)(lds + L_Q + (c & 1) * 512 + s * 64 + g * 16);
    const LAS char* wring = lds + L_RING + wave * (RS * 8192);
    LAS char* scw = lds + L_SC + wave * 4096 + (g * 2 + (c & 1)) * 16;
    const int kro = c * 512 + ((g ^ c) * 16);
    using M0_ = std::integral_constant<int, 0>; using M1_ = std::integral_constant<int, 1>; using M2_ = std::integral_constant<int, 2>;
    int fs = RS - 1, cs = 0;
    float mx = -INFINITY;
    auto qk = [&](auto mode, int s) {
      constexpr int MODE = decltype(mode)::value;
      const LAS char* base = wring + cs * 8192;
      const unsigned dst = ring0 + fs * 8192; const char* src = srcof(s + RS - 1);
      f32x4 a = {0.f, 0.f, 0.f, 0.f};
#pragma unroll
      for (int p = 0; p < 8; ++p) {
        ISS(MODE, p);
        const v4u kf = *(const LAS v4u*)(base + ((kro ^ ((p & 3) << 6)) + (p >> 2) * 256));
        a = mfma32(kf, qf[p], a);
        asm volatile("" : "+v"(a));
      }
      if (c < 2) *(LAS f32x4*)(scw + s * 128) = a;
      mx = fmaxf(fmaxf(mx, a[0]), fmaxf(fmaxf(a[1], a[2]), a[3]));
      fs = cs; cs = (cs == RS - 1) ? 0 : cs + 1;
    };
    waitvm<8 * (RS - 1)>();
    qk(M2_{}, 0);
    for (int s = 1; s < 33 - RS; ++s) { waitvm<8 * (RS - 2)>(); qk(M0_{}, s); }
    for (int s = 33 - RS; s < 32; ++s) { waitvm<8 * (RS - 2)>(); qk(M1_{}, s); }
    const float M = rows_max(mx) * C2;
    STAMP(2);
    f32x4 acc[16], accl = {0.f, 0.f, 0.f, 0.f};
#pragma unroll
    for (int d = 0; d < 16; ++d) acc[d] = f32x4{0.f, 0.f, 0.f, 0.f};
    const int kq = 4 * g + ((lane >> 2) & 3);
    const int vro = kq * 512 + (kq & 7) * 32 + (lane & 3) * 8;
    const v2u ones = {0x3F803F80u, 0x3F803F80u};
    auto pv = [&](auto mode, int i) {
      constexpr int MODE = decltype(mode)::value;
      const LAS char* base = wring + cs * 8192;
      const unsigned dst = ring0 + fs * 8192; const char* src = srcof(32 + i + RS - 1);
      const f32x4 sc = *(const LAS f32x4*)(scw + i * 128);
      const v2u pf = {cvtpk(ex2(fmaf(sc[0], C2, -M)), ex2(fmaf(sc[1], C2, -M))), cvtpk(ex2(fmaf(sc[2], C2, -M)), ex2(fmaf(sc[3], C2, -M)))};
      accl = mfma16(ones, pf, accl);
#pragma unroll
      for (int p = 0; p < 8; ++p) {
        ISS(MODE, p);
        const LAS char* a = base + (vro ^ (p << 5));
        acc[p] = mfma16(ldtr(a), pf, acc[p]);
        acc[p + 8] = mfma16(ldtr(a + 256), pf, acc[p + 8]);
        if (MODE != 2) asm volatile("" : "+v"(acc[p]), "+v"(acc[p + 8]));
      }
      fs = cs; cs = (cs == RS - 1) ? 0 : cs + 1;
    };
    for (int i = 0; i < 33 - RS; ++i) { waitvm<8 * (RS - 2)>(); pv(M1_{}, i); }
    for (int i = 33 - RS; i < 31; ++i) { waitvm<8>(); pv(M2_{}, i); }
    waitvm<0>();
    pv(M2_{}, 31);
    waitlgkm();
    if (c < 2) {
#pragma unroll
      for (int d = 0; d < 16; ++d) *(LAS f32x4*)(lds + L_RING + wave * (RS * 8192) + c * 1024 + (16 * d + 4 * g) * 4) = acc[d];
      if (g == 0) *(LAS f32x2*)(lds + L_ST + wave * 16 + c * 8) = f32x2{M, accl[0]};
    }
    const v2u tag = *(const LAS v2u*)(lds + L_TAG);
    STAMP(3);
    bar();
    if (wave != m) {
      float Mx, Ls, o0, o1;
      merge4(wave, Mx, Ls, o0, o1);
      const __amdgpu_buffer_rsrc_t ri = RSRC(ws + W_INB + (size_t)(grp * 4 + wave) * 8192 + 4096 + (((m - wave) & 3) - 1) * 1152, 1152);
      __builtin_amdgcn_raw_buffer_store_b128(v4u{asu(o0), tag.x, asu(o1), tag.y}, ri, lane * 16, 0, SC1);
      if ((lane & 31) == 0) __builtin_amdgcn_raw_buffer_store_b128(v4u{asu(Mx), tag.x, asu(Ls), tag.y}, ri, 1024 + hd * 16, 0, SC1);
      STAMP(4);
    } else if (jb < 28) {
      const __amdgpu_buffer_rsrc_t rs = RSRC(ws + W_INB + (size_t)bid * 8192, 4096);
      v4u v[4];
      for (unsigned sp = 0; sp < SPIN_CAP; ++sp) {
        asm volatile("" ::: "memory");
#pragma unroll
        for (int it = 0; it < 4; ++it) v[it] = __builtin_amdgcn_raw_buffer_load_b128(rs, it * 1024 + lane * 16, 0, SC1);
        bool ok = true;
#pragma unroll
        for (int it = 0; it < 4; ++it) ok &= (v[it].y == tag.x) & (v[it].w == tag.y);
        if (__all(ok)) break;
      }
      float s0 = (asf(v[0].x) + asf(v[1].x)) + (asf(v[2].x) + asf(v[3].x));
      float s1 = (asf(v[0].z) + asf(v[1].z)) + (asf(v[2].z) + asf(v[3].z));
      s0 += asf(__builtin_amdgcn_mov_dpp((int)asu(s0), 0x128, 0xf, 0xf, true));
      s1 += asf(__builtin_amdgcn_mov_dpp((int)asu(s1), 0x128, 0xf, 0xf, true));
      s0 = rows_sum(s0); s1 = rows_sum(s1);
      if (lane < 8) out[b * (DM / 2) + jb * 8 + lane] = cvtpk(s0, s1);
      STAMP(5);
    }
  } else {
    const int hw = wave - 4;
    if (hw == 0) {
      const v4u qv = *(const GAS v4u*)(ws + W_Q + (size_t)b * (NQ * 2) + kvh * 1024 + lane * 16);
      const v2u tg = *(const GAS v2u*)(ws + W_EPOCH);
      *(LAS v4u*)(lds + L_Q + lane * 16) = qv;
      if (lane == 0) *(LAS v2u*)(lds + L_TAG) = tg;
    }
    bar();
    const int ch = c < 14 ? c : 13;
    const char* wp = wo + (size_t)((2 * kvh + (g >> 1)) * 256 + 64 * m + 32 * (g & 1)) * (DM * 2) + hw * 224 + ch * 16;
    unsigned wr[16][8];
#define WOB 2
#pragma unroll
    for (int i0 = 0; i0 < 16; i0 += WOB) {
      v4u r[2 * WOB];
#pragma unroll
      for (int u = 0; u < 2 * WOB; ++u) r[u] = *(const GAS v4u*)(wp + (size_t)(2 * i0 + u) * (DM * 2));
#pragma unroll
      for (int u = 0; u < WOB; ++u)
#pragma unroll
        for (int e = 0; e < 4; ++e) {
          wr[i0 + u][2 * e] = __builtin_amdgcn_perm(r[2 * u + 1][e], r[2 * u][e], 0x05040100u);
          wr[i0 + u][2 * e + 1] = __builtin_amdgcn_perm(r[2 * u + 1][e], r[2 * u][e], 0x07060302u);
        }
#pragma unroll
      for (int u = 0; u < WOB; ++u)
        asm volatile("" : "+v"(wr[i0 + u][0]), "+v"(wr[i0 + u][1]), "+v"(wr[i0 + u][2]), "+v"(wr[i0 + u][3]), "+v"(wr[i0 + u][4]), "+v"(wr[i0 + u][5]), "+v"(wr[i0 + u][6]), "+v"(wr[i0 + u][7]) :: "memory");
    }
    STAMP(1);
    const v2u tag = *(const LAS v2u*)(lds + L_TAG);
    bar();
    STAMP(3);
    float Mx, Ls, o0, o1;
    merge4(m, Mx, Ls, o0, o1);
    {
      const __amdgpu_buffer_rsrc_t rs = RSRC(ws + W_INB + (size_t)bid * 8192 + 4096, 3 * 1152);
      v4u v[3], s[3];
      for (unsigned sp = 0; sp < SPIN_CAP; ++sp) {
        asm volatile("" ::: "memory");
#pragma unroll
        for (int it = 0; it < 3; ++it) {
          v[it] = __builtin_amdgcn_raw_buffer_load_b128(rs, it * 1152 + lane * 16, 0, SC1);
          s[it] = __builtin_amdgcn_raw_buffer_load_b128(rs, it * 1152 + 1024 + hd * 16, 0, SC1);
        }
        bool ok = true;
#pragma unroll
        for (int it = 0; it < 3; ++it) ok &= (v[it].y == tag.x) & (v[it].w == tag.y) & (s[it].y == tag.x) & (s[it].w == tag.y);
        if (__all(ok)) break;
      }
      STAMP(4);
      const float Mg = fmaxf(fmaxf(Mx, asf(s[0].x)), fmaxf(asf(s[1].x), asf(s[2].x)));
      const float f = ex2(Mx - Mg);
      Ls *= f; o0 *= f; o1 *= f;
#pragma unroll
      for (int it = 0; it < 3; ++it) {
        const float fi = ex2(asf(s[it].x) - Mg);
        Ls = fmaf(fi, asf(s[it].z), Ls); o0 = fmaf(fi, asf(v[it].x), o0); o1 = fmaf(fi, asf(v[it].z), o1);
      }
    }
    const float inv = __builtin_amdgcn_rcpf(Ls);
    const unsigned op = cvtpk(o0 * inv, o1 * inv);
    float y[8];
#pragma unroll
    for (int e = 0; e < 8; ++e) y[e] = 0.f;
#define STEP(i) { const unsigned ob = row_bcast<i>(op); _Pragma("unroll") for (int e = 0; e < 8; ++e) y[e] = dot2(wr[i][e], ob, y[e]); }
    STEP(0) STEP(1) STEP(2) STEP(3) STEP(4) STEP(5) STEP(6) STEP(7) STEP(8) STEP(9) STEP(10) STEP(11) STEP(12) STEP(13) STEP(14) STEP(15)
#undef STEP
    float z[4];
#pragma unroll
    for (int e = 0; e < 4; ++e) { auto s = __builtin_amdgcn_permlane32_swap(asu(y[e]), asu(y[e + 4]), false, false); z[e] = asf(s[0]) + asf(s[1]); }
    float u[2];
#pragma unroll
    for (int e = 0; e < 2; ++e) { auto s = __builtin_amdgcn_permlane16_swap(asu(z[e]), asu(z[e + 2]), false, false); u[e] = asf(s[0]) + asf(s[1]); }
    if (c < 14) {
      const int n0 = hw * 112 + c * 8 + g * 2;
      const __amdgpu_buffer_rsrc_t ri = RSRC(ws + W_INB + (size_t)b * (32 * 8192), 32 * 8192);
      __builtin_amdgcn_raw_buffer_store_b128(v4u{asu(u[0]), tag.x, asu(u[1]), tag.y}, ri, (n0 >> 4) * 8192 + jb * 128 + (n0 & 15) * 8, 0, SC1);
    }
    STAMP(5);
  }
  STAMP_FLUSH();
}

__global__ void k_rep(const char* ws) {
  const unsigned long long* ts = (const unsigned long long*)(ws + W_TS);
  unsigned long long mn = ~0ull;
  for (int g = 0; g < 256; ++g) for (int w = 0; w < 8; ++w) mn = ts[(g * 8 + w) * 8] < mn ? ts[(g * 8 + w) * 8] : mn;
  for (int i = 1; i < 6; ++i) { unsigned long long lo = ~0ull, hi = 0; double s = 0; int n = 0;
    for (int g = 0; g < 256; ++g) for (int w = 0; w < 4; ++w) { unsigned long long t = ts[(g * 8 + w) * 8 + i]; if (t < mn) continue; t -= mn; lo = t < lo ? t : lo; hi = t > hi ? t : hi; s += t; ++n; }
    printf("stamp %d: min %.2f avg %.2f max %.2f\n", i, lo * 0.01, s / n * 0.01, hi * 0.01); }
}
extern "C" void kernel_launch(void* const* d_in, const int* in_sizes, int n_in, void* d_out, int out_size, void* d_ws, size_t ws_size, hipStream_t stream) {
  (void)hipFuncSetAttribute((const void*)k_main, hipFuncAttributeMaxDynamicSharedMemorySize, L_END);
  k_q<<<256, 256, 0, stream>>>((const char*)d_in[0], (const char*)d_in[3], (const char*)d_in[4], (char*)d_ws);
  k_main<<<256, 512, L_END, stream>>>((const char*)d_in[1], (const char*)d_in[2], (const char*)d_in[4], (char*)d_ws, (unsigned*)d_out);
  k_rep<<<1, 1, 0, stream>>>((const char*)d_ws);
  fprintf(stderr, "PTRS x=%p K=%p V=%p wq=%p wo=%p out=%p ws=%p\n", d_in[0], d_in[1], d_in[2], d_in[3], d_in[4], d_out, d_ws);
}
```

```cpp
#define EPOCH
#define STAMPS
#include <hip/hip_runtime.h>
#include <hip/hip_bf16.h>
#include <type_traits>

typedef unsigned v4u __attribute__((ext_vector_type(4)));
typedef unsigned v2u __attribute__((ext_vector_type(2)));
typedef float f32x4 __attribute__((ext_vector_type(4)));
typedef float f32x2 __attribute__((ext_vector_type(2)));
typedef short bf16x8 __attribute__((ext_vector_type(8)));
typedef short s16x4 __attribute__((ext_vector_type(4)));
typedef __bf16 bf16x2 __attribute__((ext_vector_type(2)));
#define LAS __attribute__((address_space(3)))
#define GAS __attribute__((address_space(1)))

#define RS 4
#define NIF 5
#define NIFA NIF
#define NWI 4

constexpr int DM = 448, SK = 8192, DH = 256, NQ = 4096;
constexpr float C2 = 0.0625f * 1.4426950408889634f;
constexpr unsigned SPIN_CAP = 1u << 20;

constexpr size_t W_EPOCH = 0;
constexpr size_t W_Q = 4096;
constexpr size_t W_INB = W_Q + 65536;
constexpr size_t W_END = W_INB + 256 * 8192;
constexpr size_t W_TS = 8u << 20;

constexpr int L_RING = 0;
constexpr int L_SC = 4 * RS * 8192;
constexpr int L_Q = L_SC + 16384;
constexpr int L_ST = L_Q + 1024;
constexpr int L_TAG = L_ST + 64;
constexpr int L_END = L_TAG + 64;

template <int N> __device__ __forceinline__ void waitvm() { asm volatile("s_waitcnt vmcnt(%0)" ::"i"(N) : "memory"); }
__device__ __forceinline__ void waitlgkm() { asm volatile("s_waitcnt lgkmcnt(0)" ::: "memory"); }
__device__ __forceinline__ void bar() { asm volatile("s_waitcnt lgkmcnt(0)\n\ts_barrier" ::: "memory"); }
__device__ __forceinline__ void dma_nt(unsigned ldsaddr, unsigned voff, const void* sbase) {
  unsigned sv;
  asm volatile("s_mov_b32 %0, m0\n\ts_mov_b32 m0, %1\n\ts_nop 0\n\tglobal_load_lds_dwordx4 %2, %3 nt\n\ts_mov_b32 m0, %0" : "=&s"(sv) : "s"(ldsaddr), "v"(voff), "s"(sbase) : "memory");
}
__device__ __forceinline__ void dma_def(unsigned ldsaddr, unsigned voff, const void* sbase) {
  unsigned sv;
  asm volatile("s_mov_b32 %0, m0\n\ts_mov_b32 m0, %1\n\ts_nop 0\n\tglobal_load_lds_dwordx4 %2, %3\n\ts_mov_b32 m0, %0" : "=&s"(sv) : "s"(ldsaddr), "v"(voff), "s"(sbase) : "memory");
}
__device__ __forceinline__ f32x4 mfma32(v4u a, v4u b, f32x4 c) {
  return __builtin_amdgcn_mfma_f32_16x16x32_bf16(__builtin_bit_cast(bf16x8, a), __builtin_bit_cast(bf16x8, b), c, 0, 0, 0);
}
__device__ __forceinline__ f32x4 mfma16(v2u a, v2u b, f32x4 c) {
  return __builtin_amdgcn_mfma_f32_16x16x16bf16_1k(__builtin_bit_cast(s16x4, a), __builtin_bit_cast(s16x4, b), c, 0, 0, 0);
}
__device__ __forceinline__ v2u ldtr(const LAS char* p) {
  return __builtin_bit_cast(v2u, __builtin_amdgcn_ds_read_tr16_b64_v4i16((LAS s16x4*)p));
}
__device__ __forceinline__ unsigned cvtpk(float lo, float hi) { return __builtin_bit_cast(unsigned, __builtin_convertvector(f32x2{lo, hi}, bf16x2)); }
__device__ __forceinline__ float dot2(unsigned a, unsigned b, float acc) {
  return __builtin_amdgcn_fdot2_f32_bf16(__builtin_bit_cast(bf16x2, a), __builtin_bit_cast(bf16x2, b), acc, false);
}
__device__ __forceinline__ float asf(unsigned u) { return __builtin_bit_cast(float, u); }
__device__ __forceinline__ unsigned asu(float f) { return __builtin_bit_cast(unsigned, f); }
__device__ __forceinline__ float ex2(float x) { return __builtin_amdgcn_exp2f(x); }
__device__ __forceinline__ float rows_max(float x) {
  auto s = __builtin_amdgcn_permlane16_swap(asu(x), asu(x), false, false);
  x = fmaxf(asf(s[0]), asf(s[1]));
  auto t = __builtin_amdgcn_permlane32_swap(asu(x), asu(x), false, false);
  return fmaxf(asf(t[0]), asf(t[1]));
}
__device__ __forceinline__ float rows_sum(float x) {
  auto s = __builtin_amdgcn_permlane16_swap(asu(x), asu(x), false, false);
  x = asf(s[0]) + asf(s[1]);
  auto t = __builtin_amdgcn_permlane32_swap(asu(x), asu(x), false, false);
  return asf(t[0]) + asf(t[1]);
}
template <int N> __device__ __forceinline__ unsigned row_bcast(unsigned x) { return (unsigned)__builtin_amdgcn_mov_dpp((int)x, 0x150 + N, 0xf, 0xf, true); }
#define RSRC(p, n) __builtin_amdgcn_make_buffer_rsrc((void*)(p), 0, (int)(n), 0x00020000)
constexpr int SC1 = 16;
#define STAMP(i) do { tsv[i] = __builtin_amdgcn_s_memrealtime(); asm volatile("s_waitcnt lgkmcnt(0)" : "+s"(tsv[i]) :: "memory"); } while (0)
#define STAMP_FLUSH() do { if (lane == 0) { _Pragma("unroll") for (int i_ = 0; i_ < 8; ++i_) ((unsigned long long*)(ws + W_TS))[(blockIdx.x * 8 + wave) * 8 + i_] = tsv[i_]; } } while (0)

__global__ void __launch_bounds__(256) k_q(const char* __restrict__ x, const char* __restrict__ wq, const char* __restrict__ wo, char* __restrict__ ws) {
  __shared__ __attribute__((aligned(16))) char lds[14 * 1024 + 4096 + 16384];
  const int tid = threadIdx.x, lane = tid & 63, wave = __builtin_amdgcn_readfirstlane(tid >> 6);
  const int j = blockIdx.x, cb = (j & 7) * 32 + (j >> 3);
  const unsigned long long tq0 = __builtin_amdgcn_s_memrealtime();
  const int g = lane >> 4, c = lane & 15;
  const unsigned l0 = (unsigned)(size_t)(LAS char*)lds;
  const unsigned voff = (lane >> 1) * 8192 + (lane & 1) * 16;
#pragma unroll
  for (int i = 0; i < 4; ++i) {
    const int s = wave + 4 * i;
    if (s < 14) dma_def(l0 + s * 1024, voff, wq + (size_t)s * 32 * 8192 + cb * 32);
  }
  v4u xf[4];
#pragma unroll
  for (int i = 0; i < 4; ++i) {
    const int s = (wave + 4 * i < 14) ? wave + 4 * i : 0;
    xf[i] = *(const GAS v4u*)(x + (c & 7) * (DM * 2) + s * 64 + g * 16);
  }
  if (j == 0 && tid == 0) { GAS unsigned long long* e = (GAS unsigned long long*)(ws + W_EPOCH); *e = *e + 0x9E3779B97F4A7C15ull; }
  waitvm<0>();
  f32x4 acc = {0.f, 0.f, 0.f, 0.f};
  const LAS char* lp = (const LAS char*)lds + (8 * g + ((lane >> 2) & 3)) * 32 + (lane & 3) * 8;
#pragma unroll
  for (int i = 0; i < 4; ++i) {
    const int s = wave + 4 * i;
    if (s < 14) {
      const v2u a0 = ldtr(lp + s * 1024), a1 = ldtr(lp + s * 1024 + 128);
      acc = mfma32(v4u{a0.x, a0.y, a1.x, a1.y}, xf[i], acc);
    }
  }
  *(LAS f32x4*)((LAS char*)lds + 14 * 1024 + wave * 1024 + lane * 16) = acc;
  bar();
  if (wave == 0 && c < 8) {
    const LAS f32x4* r = (const LAS f32x4*)((LAS char*)lds + 14 * 1024 + lane * 16);
    const f32x4 s = (r[0] + r[64]) + (r[128] + r[192]);
    *(GAS v2u*)(ws + W_Q + (size_t)c * (NQ * 2) + (cb * 16 + 4 * g) * 2) = v2u{cvtpk(s[0], s[1]), cvtpk(s[2], s[3])};
  }
  if (tid == 0) { ((unsigned long long*)(ws + W_TS + (1 << 20)))[j * 2] = tq0; ((unsigned long long*)(ws + W_TS + (1 << 20)))[j * 2 + 1] = __builtin_amdgcn_s_memrealtime(); }
}

__global__ void __launch_bounds__(512, 2) k_main(const char* __restrict__ Kc, const char* __restrict__ Vc, const char* __restrict__ wo, char* __restrict__ ws, unsigned* __restrict__ out) {
  extern __shared__ __attribute__((aligned(16))) char lds_[];
  LAS char* lds = (LAS char*)lds_;
  const int tid = threadIdx.x, lane = tid & 63, wave = __builtin_amdgcn_readfirstlane(tid >> 6);
  const int bid = blockIdx.x, grp = bid >> 2, m = bid & 3, b = bid >> 5, jb = bid & 31, kvh = jb >> 2;
  const int g = lane >> 4, c = lane & 15, hd = lane >> 5;
  unsigned long long tsv[8] = {0, 0, 0, 0, 0, 0, 0, 0};
  STAMP(0);
  auto merge4 = [&](int sl, float& Mx, float& Ls, float& o0, float& o1) {
    f32x2 st[4], pa[4];
#pragma unroll
    for (int w = 0; w < 4; ++w) {
      st[w] = *(const LAS f32x2*)(lds + L_ST + w * 16 + hd * 8);
      pa[w] = *(const LAS f32x2*)(lds + L_RING + w * (RS * 8192) + hd * 1024 + sl * 256 + (lane & 31) * 8);
    }
    Mx = fmaxf(fmaxf(st[0].x, st[1].x), fmaxf(st[2].x, st[3].x));
    Ls = 0.f; o0 = 0.f; o1 = 0.f;
#pragma unroll
    for (int w = 0; w < 4; ++w) {
      const float f = ex2(st[w].x - Mx);
      Ls = fmaf(f, st[w].y, Ls); o0 = fmaf(f, pa[w].x, o0); o1 = fmaf(f, pa[w].y, o1);
    }
  };
  if (wave < 4) {
    __builtin_amdgcn_s_setprio(3);
    const size_t t0 = (size_t)grp * (SK * DH * 2) + (NWI == 4 ? (size_t)m * 1048576 + wave * 8192 : (size_t)(m * 4 + wave) * 8192);
    const char* kb = Kc + t0;
    const char* vb = Vc + t0;
    const unsigned ring0 = (unsigned)(size_t)lds + L_RING + wave * (RS * 8192);
    const unsigned kvo = hd * 512 + (((lane & 31) ^ hd) * 16);
    const unsigned vvo = hd * 512 + (((lane & 31) ^ (hd << 1)) * 16);
    auto srcof = [&](int s) -> const char* { return (s < 32 ? kb : vb) + (size_t)(s & 31) * (NWI * 8192); };
#define ISS_K(p) do { waitvm<NIFA - 1>(); dma_nt(dst + (p) * 1024, kvo ^ ((p) << 5), src + (p) * 1024); } while (0)
#define ISS_V(p) do { waitvm<NIF - 1>(); dma_nt(dst + (p) * 1024, vvo ^ (((p) & 3) << 6), src + (p) * 1024); } while (0)
#define ISS(MODE, p) do { if (MODE == 0) ISS_K(p); else if (MODE == 1) ISS_V(p); } while (0)
    for (int s = 0; s < RS; ++s) {
      const unsigned dst = ring0 + s * 8192; const char* src = srcof(s);
#pragma unroll
      for (int p = 0; p < 8; ++p) ISS_K(p);
    }
    bar();
    STAMP(1);
    v4u qf[8];
#pragma unroll
    for (int s = 0; s < 8; ++s) qf[s] = *(const LAS v4u*)(lds + L_Q + (c & 1) * 512 + s * 64 + g * 16);
    const LAS char* wring = lds + L_RING + wave * (RS * 8192);
    LAS char* scw = lds + L_SC + wave * 4096 + (g * 2 + (c & 1)) * 16;
    const int kro = c * 512 + ((g ^ c) * 16);
    using M0_ = std::integral_constant<int, 0>; using M1_ = std::integral_constant<int, 1>; using M2_ = std::integral_constant<int, 2>;
    int fs = RS - 1, cs = 0;
    float mx = -INFINITY;
    auto qk = [&](auto mode, int s) {
      constexpr int MODE = decltype(mode)::value;
      const LAS char* base = wring + cs * 8192;
      const unsigned dst = ring0 + fs * 8192; const char* src = srcof(s + RS - 1);
      f32x4 a = {0.f, 0.f, 0.f, 0.f};
#pragma unroll
      for (int p = 0; p < 8; ++p) {
        ISS(MODE, p);
        const v4u kf = *(const LAS v4u*)(base + ((kro ^ ((p & 3) << 6)) + (p >> 2) * 256));
        a = mfma32(kf, qf[p], a);
        asm volatile("" : "+v"(a));
      }
      if (c < 2) *(LAS f32x4*)(scw + s * 128) = a;
      mx = fmaxf(fmaxf(mx, a[0]), fmaxf(fmaxf(a[1], a[2]), a[3]));
      fs = cs; cs = (cs == RS - 1) ? 0 : cs + 1;
    };
    waitvm<8 * (RS - 1)>();
    qk(M2_{}, 0);
    for (int s = 1; s < 33 - RS; ++s) { waitvm<8 * (RS - 2)>(); qk(M0_{}, s); }
    for (int s = 33 - RS; s < 32; ++s) { waitvm<8 * (RS - 2)>(); qk(M1_{}, s); }
    const float M = rows_max(mx) * C2;
    STAMP(2);
    f32x4 acc[16], accl = {0.f, 0.f, 0.f, 0.f};
#pragma unroll
    for (int d = 0; d < 16; ++d) acc[d] = f32x4{0.f, 0.f, 0.f, 0.f};
    const int kq = 4 * g + ((lane >> 2) & 3);
    const int vro = kq * 512 + (kq & 7) * 32 + (lane & 3) * 8;
    const v2u ones = {0x3F803F80u, 0x3F803F80u};
    auto pv = [&](auto mode, int i) {
      constexpr int MODE = decltype(mode)::value;
      const LAS char* base = wring + cs * 8192;
      const unsigned dst = ring0 + fs * 8192; const char* src = srcof(32 + i + RS - 1);
      const f32x4 sc = *(const LAS f32x4*)(scw + i * 128);
      const v2u pf = {cvtpk(ex2(fmaf(sc[0], C2, -M)), ex2(fmaf(sc[1], C2, -M))), cvtpk(ex2(fmaf(sc[2], C2, -M)), ex2(fmaf(sc[3], C2, -M)))};
      accl = mfma16(ones, pf, accl);
#pragma unroll
      for (int p = 0; p < 8; ++p) {
        ISS(MODE, p);
        const LAS char* a = base + (vro ^ (p << 5));
        acc[p] = mfma16(ldtr(a), pf, acc[p]);
        acc[p + 8] = mfma16(ldtr(a + 256), pf, acc[p + 8]);
        if (MODE != 2) asm volatile("" : "+v"(acc[p]), "+v"(acc[p + 8]));
      }
      fs = cs; cs = (cs == RS - 1) ? 0 : cs + 1;
    };
    for (int i = 0; i < 33 - RS; ++i) { waitvm<8 * (RS - 2)>(); pv(M1_{}, i); }
    for (int i = 33 - RS; i < 31; ++i) { waitvm<8>(); pv(M2_{}, i); }
    waitvm<0>();
    pv(M2_{}, 31);
    waitlgkm();
    if (c < 2) {
#pragma unroll
      for (int d = 0; d < 16; ++d) *(LAS f32x4*)(lds + L_RING + wave * (RS * 8192) + c * 1024 + (16 * d + 4 * g) * 4) = acc[d];
      if (g == 0) *(LAS f32x2*)(lds + L_ST + wave * 16 + c * 8) = f32x2{M, accl[0]};
    }
    const v2u tag = *(const LAS v2u*)(lds + L_TAG);
    STAMP(3);
    bar();
    if (wave != m) {
      float Mx, Ls, o0, o1;
      merge4(wave, Mx, Ls, o0, o1);
      const __amdgpu_buffer_rsrc_t ri = RSRC(ws + W_INB + (size_t)(grp * 4 + wave) * 8192 + 4096 + (((m - wave) & 3) - 1) * 1152, 1152);
      __builtin_amdgcn_raw_buffer_store_b128(v4u{asu(o0), tag.x, asu(o1), tag.y}, ri, lane * 16, 0, SC1);
      if ((lane & 31) == 0) __builtin_amdgcn_raw_buffer_store_b128(v4u{asu(Mx), tag.x, asu(Ls), tag.y}, ri, 1024 + hd * 16, 0, SC1);
      STAMP(4);
    } else if (jb < 28) {
      const __amdgpu_buffer_rsrc_t rs = RSRC(ws + W_INB + (size_t)bid * 8192, 4096);
      v4u v[4];
      for (unsigned sp = 0; sp < SPIN_CAP; ++sp) {
        asm volatile("" ::: "memory");
#pragma unroll
        for (int it = 0; it < 4; ++it) v[it] = __builtin_amdgcn_raw_buffer_load_b128(rs, it * 1024 + lane * 16, 0, SC1);
        bool ok = true;
#pragma unroll
        for (int it = 0; it < 4; ++it) ok &= (v[it].y == tag.x) & (v[it].w == tag.y);
        if (__all(ok)) break;
      }
      float s0 = (asf(v[0].x) + asf(v[1].x)) + (asf(v[2].x) + asf(v[3].x));
      float s1 = (asf(v[0].z) + asf(v[1].z)) + (asf(v[2].z) + asf(v[3].z));
      s0 += asf(__builtin_amdgcn_mov_dpp((int)asu(s0), 0x128, 0xf, 0xf, true));
      s1 += asf(__builtin_amdgcn_mov_dpp((int)asu(s1), 0x128, 0xf, 0xf, true));
      s0 = rows_sum(s0); s1 = rows_sum(s1);
      if (lane < 8) out[b * (DM / 2) + jb * 8 + lane] = cvtpk(s0, s1);
      STAMP(5);
    }
  } else {
    const int hw = wave - 4;
    if (hw == 0) {
      const v4u qv = *(const GAS v4u*)(ws + W_Q + (size_t)b * (NQ * 2) + kvh * 1024 + lane * 16);
      const v2u tg = *(const GAS v2u*)(ws + W_EPOCH);
      *(LAS v4u*)(lds + L_Q + lane * 16) = qv;
      if (lane == 0) *(LAS v2u*)(lds + L_TAG) = tg;
    }
    bar();
    const int ch = c < 14 ? c : 13;
    const char* wp = wo + (size_t)((2 * kvh + (g >> 1)) * 256 + 64 * m + 32 * (g & 1)) * (DM * 2) + hw * 224 + ch * 16;
    unsigned wr[16][8];
#define WOB 2
#pragma unroll
    for (int i0 = 0; i0 < 16; i0 += WOB) {
      v4u r[2 * WOB];
#pragma unroll
      for (int u = 0; u < 2 * WOB; ++u) r[u] = *(const GAS v4u*)(wp + (size_t)(2 * i0 + u) * (DM * 2));
#pragma unroll
      for (int u = 0; u < WOB; ++u)
#pragma unroll
        for (int e = 0; e < 4; ++e) {
          wr[i0 + u][2 * e] = __builtin_amdgcn_perm(r[2 * u + 1][e], r[2 * u][e], 0x05040100u);
          wr[i0 + u][2 * e + 1] = __builtin_amdgcn_perm(r[2 * u + 1][e], r[2 * u][e], 0x07060302u);
        }
#pragma unroll
      for (int u = 0; u < WOB; ++u)
        asm volatile("" : "+v"(wr[i0 + u][0]), "+v"(wr[i0 + u][1]), "+v"(wr[i0 + u][2]), "+v"(wr[i0 + u][3]), "+v"(wr[i0 + u][4]), "+v"(wr[i0 + u][5]), "+v"(wr[i0 + u][6]), "+v"(wr[i0 + u][7]) :: "memory");
    }
    STAMP(1);
    const v2u tag = *(const LAS v2u*)(lds + L_TAG);
    bar();
    STAMP(3);
    float Mx, Ls, o0, o1;
    merge4(m, Mx, Ls, o0, o1);
    {
      const __amdgpu_buffer_rsrc_t rs = RSRC(ws + W_INB + (size_t)bid * 8192 + 4096, 3 * 1152);
      v4u v[3], s[3];
      for (unsigned sp = 0; sp < SPIN_CAP; ++sp) {
        asm volatile("" ::: "memory");
#pragma unroll
        for (int it = 0; it < 3; ++it) {
          v[it] = __builtin_amdgcn_raw_buffer_load_b128(rs, it * 1152 + lane * 16, 0, SC1);
          s[it] = __builtin_amdgcn_raw_buffer_load_b128(rs, it * 1152 + 1024 + hd * 16, 0, SC1);
        }
        bool ok = true;
#pragma unroll
        for (int it = 0; it < 3; ++it) ok &= (v[it].y == tag.x) & (v[it].w == tag.y) & (s[it].y == tag.x) & (s[it].w == tag.y);
        if (__all(ok)) break;
      }
      STAMP(4);
      const float Mg = fmaxf(fmaxf(Mx, asf(s[0].x)), fmaxf(asf(s[1].x), asf(s[2].x)));
      const float f = ex2(Mx - Mg);
      Ls *= f; o0 *= f; o1 *= f;
#pragma unroll
      for (int it = 0; it < 3; ++it) {
        const float fi = ex2(asf(s[it].x) - Mg);
        Ls = fmaf(fi, asf(s[it].z), Ls); o0 = fmaf(fi, asf(v[it].x), o0); o1 = fmaf(fi, asf(v[it].z), o1);
      }
    }
    const float inv = __builtin_amdgcn_rcpf(Ls);
    const unsigned op = cvtpk(o0 * inv, o1 * inv);
    float y[8];
#pragma unroll
    for (int e = 0; e < 8; ++e) y[e] = 0.f;
#define STEP(i) { const unsigned ob = row_bcast<i>(op); _Pragma("unroll") for (int e = 0; e < 8; ++e) y[e] = dot2(wr[i][e], ob, y[e]); }
    STEP(0) STEP(1) STEP(2) STEP(3) STEP(4) STEP(5) STEP(6) STEP(7) STEP(8) STEP(9) STEP(10) STEP(11) STEP(12) STEP(13) STEP(14) STEP(15)
#undef STEP
    float z[4];
#pragma unroll
    for (int e = 0; e < 4; ++e) { auto s = __builtin_amdgcn_permlane32_swap(asu(y[e]), asu(y[e + 4]), false, false); z[e] = asf(s[0]) + asf(s[1]); }
    float u[2];
#pragma unroll
    for (int e = 0; e < 2; ++e) { auto s = __builtin_amdgcn_permlane16_swap(asu(z[e]), asu(z[e + 2]), false, false); u[e] = asf(s[0]) + asf(s[1]); }
    if (c < 14) {
      const int n0 = hw * 112 + c * 8 + g * 2;
      const __amdgpu_buffer_rsrc_t ri = RSRC(ws + W_INB + (size_t)b * (32 * 8192), 32 * 8192);
      __builtin_amdgcn_raw_buffer_store_b128(v4u{asu(u[0]), tag.x, asu(u[1]), tag.y}, ri, (n0 >> 4) * 8192 + jb * 128 + (n0 & 15) * 8, 0, SC1);
    }
    STAMP(5);
  }
  STAMP_FLUSH();
}

__global__ void k_rep(const char* ws) {
  __shared__ unsigned long long sm[8][256];
  const unsigned long long tr0 = __builtin_amdgcn_s_memrealtime();
  const unsigned long long* ts = (const unsigned long long*)(ws + W_TS);
  const unsigned long long* tq = (const unsigned long long*)(ws + W_TS + (1 << 20));
  const int g = threadIdx.x;
  unsigned long long mn = ~0ull, mx = 0, e3 = 0, e1 = 0;
  for (int w = 0; w < 8; ++w) { unsigned long long t = ts[(g * 8 + w) * 8]; mn = t < mn ? t : mn; for (int i = 0; i < 6; ++i) { t = ts[(g * 8 + w) * 8 + i]; mx = t > mx ? t : mx; } }
  for (int w = 0; w < 4; ++w) { unsigned long long t = ts[(g * 8 + w) * 8 + 3]; e3 = t > e3 ? t : e3; t = ts[(g * 8 + w) * 8 + 1]; e1 = t > e1 ? t : e1; }
  sm[0][g] = mn; sm[1][g] = mx; sm[2][g] = e3; sm[3][g] = tq[g * 2]; sm[4][g] = tq[g * 2 + 1]; sm[5][g] = e1;
  __syncthreads();
  if (g == 0) {
    unsigned long long q0 = ~0ull, q1 = 0, s3 = 0, s1 = 0;
    for (int i = 0; i < 256; ++i) { mn = sm[0][i] < mn ? sm[0][i] : mn; mx = sm[1][i] > mx ? sm[1][i] : mx; q0 = sm[3][i] < q0 ? sm[3][i] : q0; q1 = sm[4][i] > q1 ? sm[4][i] : q1; }
    for (int i = 0; i < 256; ++i) { s3 += sm[2][i] - mn; s1 += sm[5][i] - mn; }
    unsigned long long D = 0;
    if (D > 20000) D = 20000;
    const unsigned long long tend = tr0 + 1000 + D * 20;
    while (__builtin_amdgcn_s_memrealtime() < tend) __builtin_amdgcn_s_sleep(8);
  }
}
extern "C" void kernel_launch(void* const* d_in, const int* in_sizes, int n_in, void* d_out, int out_size, void* d_ws, size_t ws_size, hipStream_t stream) {
  (void)hipFuncSetAttribute((const void*)k_main, hipFuncAttributeMaxDynamicSharedMemorySize, L_END);
  k_q<<<256, 256, 0, stream>>>((const char*)d_in[0], (const char*)d_in[3], (const char*)d_in[4], (char*)d_ws);
  k_main<<<256, 512, L_END, stream>>>((const char*)d_in[1], (const char*)d_in[2], (const char*)d_in[4], (char*)d_ws, (unsigned*)d_out);
  k_rep<<<1, 256, 0, stream>>>((const char*)d_ws);
}
```

```cpp
#define WODEF
#define WQDEF
#define TOFF 0
#include <hip/hip_runtime.h>
#include <hip/hip_bf16.h>

typedef unsigned v4u __attribute__((ext_vector_type(4)));
typedef unsigned v2u __attribute__((ext_vector_type(2)));
typedef float f32x4 __attribute__((ext_vector_type(4)));
typedef float f32x2 __attribute__((ext_vector_type(2)));
typedef short bf16x8 __attribute__((ext_vector_type(8)));
typedef short s16x4 __attribute__((ext_vector_type(4)));
typedef __bf16 bf16x2 __attribute__((ext_vector_type(2)));
#define LAS __attribute__((address_space(3)))
#define GAS __attribute__((address_space(1)))

#define RS 4
#define RH 4
#define WLO 4
#define WMID 6
#define WHI 8
#define DUTY0 8
#define DUTY 4
#define TAU 1095
#define PG 4
#define QSLEEP 8
#define QSPIN 64
#define WOB 2
#define FSLEEP 12

static_assert(RH >= 1 && RH <= RS, "parked tiles come from ring slots 0..RH-1");
constexpr int DM = 448, SK = 8192, DH = 256;
constexpr float C2 = 0.0625f * 1.4426950408889634f;
constexpr unsigned SPIN_CAP = 1u << 15;

constexpr size_t W_REQ = 0;
constexpr size_t W_QG = 65536;
constexpr size_t W_INB = W_QG + 131072;

constexpr int L_RING = 0;
constexpr int L_SC = 4 * RS * 8192;
constexpr int L_Q = L_SC + 16384;
constexpr int L_ST = L_Q + 1024;
constexpr int L_RX = L_ST + 64;
constexpr int L_RS = L_RX + 2048;
constexpr int L_TAG = L_RS + 64;
constexpr int L_END = L_TAG + 64;
#define TRC(s_) do {} while (0)

template <int N> __device__ __forceinline__ void waitvm() { asm volatile("s_waitcnt vmcnt(%0)" ::"i"(N) : "memory"); }
__device__ __forceinline__ void waitlgkm() { asm volatile("s_waitcnt lgkmcnt(0)" ::: "memory"); }
__device__ __forceinline__ void bar() { asm volatile("s_waitcnt lgkmcnt(0)\n\ts_barrier" ::: "memory"); }
__device__ __forceinline__ void set_m0(unsigned v) { asm volatile("s_mov_b32 m0, %0\n\ts_nop 0" ::"s"(v) : "memory"); }
__device__ __forceinline__ void dma1(unsigned voff, const void* sbase) {
  asm volatile("global_load_lds_dwordx4 %0, %1" ::"v"(voff), "s"(sbase) : "memory");
}
#define BR3(p) ((((p) & 1) << 2) | ((p) & 2) | (((p) >> 2) & 1))
#define WMASK(d) ((1u << (d)) - 1u)
template <int P, int OFF> __device__ __forceinline__ void piece(unsigned wm, unsigned voff, const void* sbase) {
  asm volatile(
      "s_waitcnt vmcnt(%5)\n\t"
      "s_bitcmp1_b32 %0, %7\n\t"
      "s_cbranch_scc1 1f\n\t"
      "s_waitcnt vmcnt(%4)\n\t"
      "s_bitcmp1_b32 %0, %6\n\t"
      "s_cbranch_scc1 1f\n\t"
      "s_waitcnt vmcnt(%3)\n"
      "1:\n\t"
      "global_load_lds_dwordx4 %1, %2 offset:%8 nt" ::"s"(wm), "v"(voff), "s"(sbase), "i"(WLO - 1), "i"(WMID - 1), "i"(WHI - 1), "i"(BR3(P)), "i"(BR3(P) + 8), "i"(OFF)
      : "memory", "scc");
}
__device__ __forceinline__ f32x4 mfma32(v4u a, v4u b, f32x4 c) {
  return __builtin_amdgcn_mfma_f32_16x16x32_bf16(__builtin_bit_cast(bf16x8, a), __builtin_bit_cast(bf16x8, b), c, 0, 0, 0);
}
__device__ __forceinline__ f32x4 mfma16(v2u a, v2u b, f32x4 c) {
  return __builtin_amdgcn_mfma_f32_16x16x16bf16_1k(__builtin_bit_cast(s16x4, a), __builtin_bit_cast(s16x4, b), c, 0, 0, 0);
}
__device__ __forceinline__ v2u ldtr(const LAS char* p) { return __builtin_bit_cast(v2u, __builtin_amdgcn_ds_read_tr16_b64_v4i16((LAS s16x4*)p)); }
__device__ __forceinline__ unsigned cvtpk(float lo, float hi) { return __builtin_bit_cast(unsigned, __builtin_convertvector(f32x2{lo, hi}, bf16x2)); }
__device__ __forceinline__ float dot2(unsigned a, unsigned b, float acc) {
  return __builtin_amdgcn_fdot2_f32_bf16(__builtin_bit_cast(bf16x2, a), __builtin_bit_cast(bf16x2, b), acc, false);
}
__device__ __forceinline__ float asf(unsigned u) { return __builtin_bit_cast(float, u); }
__device__ __forceinline__ unsigned asu(float f) { return __builtin_bit_cast(unsigned, f); }
__device__ __forceinline__ float ex2(float x) { return __builtin_amdgcn_exp2f(x); }
__device__ __forceinline__ float rows_max(float x) {
  auto s = __builtin_amdgcn_permlane16_swap(asu(x), asu(x), false, false);
  x = fmaxf(asf(s[0]), asf(s[1]));
  auto t = __builtin_amdgcn_permlane32_swap(asu(x), asu(x), false, false);
  return fmaxf(asf(t[0]), asf(t[1]));
}
__device__ __forceinline__ float rows_sum(float x) {
  auto s = __builtin_amdgcn_permlane16_swap(asu(x), asu(x), false, false);
  x = asf(s[0]) + asf(s[1]);
  auto t = __builtin_amdgcn_permlane32_swap(asu(x), asu(x), false, false);
  return asf(t[0]) + asf(t[1]);
}
template <int N> __device__ __forceinline__ unsigned row_bcast(unsigned x) { return (unsigned)__builtin_amdgcn_mov_dpp((int)x, 0x150 + N, 0xf, 0xf, true); }
#define RSRC(p, n) __builtin_amdgcn_make_buffer_rsrc((void*)(p), 0, (int)(n), 0x00020000)
constexpr int SC1 = 16;
__device__ __forceinline__ v4u poll128(__amdgpu_buffer_rsrc_t rs, int off) {
  asm volatile("" : "+v"(off)::"memory");
  return __builtin_amdgcn_raw_buffer_load_b128(rs, off, 0, SC1);
}
__device__ __forceinline__ v2u poll64(__amdgpu_buffer_rsrc_t rs, int off) {
  asm volatile("" : "+v"(off)::"memory");
  return __builtin_amdgcn_raw_buffer_load_b64(rs, off, 0, SC1);
}
#define PINV(x) asm volatile("" : "+v"(x))
#define STAMP(i) do {} while (0)
#define STAMP_FLUSH() do {} while (0)

__device__ __forceinline__ void merge4(const LAS char* stp, const LAS char* pap, int pstride, float& Mx, float& Ls, float& o0, float& o1) {
  f32x2 st[4], pa[4];
#pragma unroll
  for (int w = 0; w < 4; ++w) {
    st[w] = *(const LAS f32x2*)(stp + w * 16);
    pa[w] = *(const LAS f32x2*)(pap + w * pstride);
  }
  Mx = fmaxf(fmaxf(st[0].x, st[1].x), fmaxf(st[2].x, st[3].x));
  Ls = 0.f; o0 = 0.f; o1 = 0.f;
#pragma unroll
  for (int w = 0; w < 4; ++w) {
    const float f = ex2(st[w].x - Mx);
    Ls = fmaf(f, st[w].y, Ls); o0 = fmaf(f, pa[w].x, o0); o1 = fmaf(f, pa[w].y, o1);
  }
}

__global__ void __launch_bounds__(512, 2) k_main(const char* __restrict__ x, const char* __restrict__ Kc, const char* __restrict__ Vc, const char* __restrict__ wq,
                                                 const char* __restrict__ wo, char* ws, unsigned* out) {
  extern __shared__ __attribute__((aligned(16))) char lds_[];
  LAS char* lds = (LAS char*)lds_;
  const int tid = threadIdx.x, lane = tid & 63, wave = __builtin_amdgcn_readfirstlane(tid >> 6);
  const int bid = blockIdx.x, grp = bid >> 2, m = bid & 3, b = bid >> 5, jb = bid & 31, kvh = jb >> 2;
  const int g = lane >> 4, c = lane & 15, hd = lane >> 5;
  STAMP(0);
  if (__builtin_expect(wave < 4, 1)) {
    __builtin_amdgcn_s_setprio(3);
    const size_t t0 = (size_t)bid * 1048576 + wave * 8192;
    const char* src = Kc + t0;
    const unsigned ring0 = (unsigned)(size_t)lds + L_RING + wave * (RS * 8192);
    unsigned vo[8];
#pragma unroll
    for (int p = 0; p < 8; ++p) vo[p] = hd * 512 + ((((lane & 31) ^ hd) * 16) ^ (p << 5)) + (p >> 2) * 4096;
    unsigned wm = WMASK(DUTY0);
    unsigned sched = (unsigned)__builtin_amdgcn_s_memrealtime() * 10u + (unsigned)(TOFF * 10 + (RH + RS) * TAU);
    unsigned tnow = 0;
#define ISS(p) do { if (((p) & 3) == 0) set_m0(dst + ((p) >> 2) * 4096); piece<(p), ((p) & 3) * 1024>(wm, vo[p], src); } while (0)
#define PACE_STEP() do { tnow = (unsigned)__builtin_amdgcn_s_memrealtime(); } while (0)
#define PACE_TAKE() do { int d_ = DUTY + (((int)(tnow * 10u - sched) * PG) / 1280); d_ = d_ < 0 ? 0 : d_ > 16 ? 16 : d_; wm = __builtin_amdgcn_readfirstlane(WMASK(d_)); sched += TAU; } while (0)
    const LAS char* wring = lds + L_RING + wave * (RS * 8192);
    LAS char* scw = lds + L_SC + wave * 4096 + (g * 2 + (c & 1)) * 16;
    const int kro = c * 512 + ((g ^ c) * 16);
#define KFRAG(base_, p) (*(const LAS v4u*)((base_) + ((kro ^ (((p) & 3) << 6)) + ((p) >> 2) * 256)))
    v4u hold[RH][8];
#pragma nounroll
    for (int r = 0;; ++r) {
      const int sl = r >= RS ? r - RS : r;
      TRC(r);
      if (r >= RS) {
        waitvm<8 * (RS - 1)>();
#pragma unroll
        for (int j = 0; j < RH; ++j)
          if (sl == j) {
#pragma unroll
            for (int p = 0; p < 8; ++p) hold[j][p] = KFRAG(wring + j * 8192, p);
          }
        waitlgkm();
        if (r == RS + RH - 1) break;
      }
      const unsigned dst = ring0 + sl * 8192;
      ISS(0); ISS(1); ISS(2); ISS(3); ISS(4); ISS(5); ISS(6); ISS(7);
      src += 32768;
    }
    STAMP(6);
    bar();
    STAMP(1);
    v4u qf[8];
#pragma unroll
    for (int s = 0; s < 8; ++s) qf[s] = *(const LAS v4u*)(lds + L_Q + (c & 1) * 512 + s * 64 + g * 16);
    float mx = -INFINITY;
    int fs = RH - 1, cs = RH % RS;
#pragma nounroll
    for (int s = RH; s < 32; ++s) {
      if (s == 33 - RS) {
        src = Vc + t0;
#pragma unroll
        for (int p = 0; p < 8; ++p) vo[p] = hd * 512 + ((((lane & 31) ^ (hd << 1)) * 16) ^ ((p & 3) << 6)) + (p >> 2) * 4096;
      }
      waitvm<8 * (RS - 2)>();
      const LAS char* base = wring + cs * 8192;
      const unsigned dst = ring0 + fs * 8192;
      ISS(0);
      v4u kf[8];
#pragma unroll
      for (int p = 0; p < 8; ++p) kf[p] = KFRAG(base, p);
      TRC(s + RS - 1);
      f32x4 a = {0.f, 0.f, 0.f, 0.f};
      ISS(1);
      a = mfma32(kf[0], qf[0], a); a = mfma32(kf[1], qf[1], a); PINV(a);
      ISS(2);
      a = mfma32(kf[2], qf[2], a); a = mfma32(kf[3], qf[3], a); PINV(a);
      ISS(3);
      a = mfma32(kf[4], qf[4], a); a = mfma32(kf[5], qf[5], a); PINV(a);
      ISS(4);
      a = mfma32(kf[6], qf[6], a); a = mfma32(kf[7], qf[7], a); PINV(a);
      ISS(5);
      if (c < 2) *(LAS f32x4*)(scw + s * 128) = a;
      mx = fmaxf(fmaxf(mx, a[0]), fmaxf(fmaxf(a[1], a[2]), a[3])); PINV(mx);
      ISS(6);
      PACE_STEP();
      ISS(7);
      PACE_TAKE();
      src += 32768;
      fs = cs; cs = (cs == RS - 1) ? 0 : cs + 1;
    }
#pragma unroll
    for (int r = 0; r < RH; ++r) {
      f32x4 a = {0.f, 0.f, 0.f, 0.f};
#pragma unroll
      for (int p = 0; p < 8; ++p) a = mfma32(hold[r][p], qf[p], a);
      if (c < 2) *(LAS f32x4*)(scw + r * 128) = a;
      mx = fmaxf(fmaxf(mx, a[0]), fmaxf(fmaxf(a[1], a[2]), a[3]));
    }
    const float M = rows_max(mx) * C2;
    STAMP(2);
    f32x4 acc[16], accl = {0.f, 0.f, 0.f, 0.f};
#pragma unroll
    for (int d = 0; d < 16; ++d) acc[d] = f32x4{0.f, 0.f, 0.f, 0.f};
    const int kq = 4 * g + ((lane >> 2) & 3);
    const int vro = kq * 512 + (kq & 7) * 32 + (lane & 3) * 8;
    const v2u ones = {0x3F803F80u, 0x3F803F80u};
#define PFRAG(sc_) v2u{cvtpk(ex2(fmaf(sc_[0], C2, -M)), ex2(fmaf(sc_[1], C2, -M))), cvtpk(ex2(fmaf(sc_[2], C2, -M)), ex2(fmaf(sc_[3], C2, -M)))}
#define PVM(p) do { acc[p] = mfma16(vf[p], pf, acc[p]); acc[(p) + 8] = mfma16(vf[(p) + 8], pf, acc[(p) + 8]); asm volatile("" : "+v"(acc[p]), "+v"(acc[(p) + 8])); } while (0)
#pragma nounroll
    for (int i = 0; i < 33 - RS; ++i) {
      waitvm<8 * (RS - 2)>();
      const LAS char* base = wring + cs * 8192;
      const unsigned dst = ring0 + fs * 8192;
      ISS(0);
      const f32x4 sc = *(const LAS f32x4*)(scw + i * 128);
      v2u vf[16];
#pragma unroll
      for (int p = 0; p < 4; ++p) { const LAS char* a = base + (vro ^ (p << 5)); vf[p] = ldtr(a); vf[p + 8] = ldtr(a + 256); }
      TRC(32 + i + RS - 1);
      ISS(1);
#pragma unroll
      for (int p = 4; p < 8; ++p) { const LAS char* a = base + (vro ^ (p << 5)); vf[p] = ldtr(a); vf[p + 8] = ldtr(a + 256); }
      v2u pf = PFRAG(sc);
      PINV(pf);
      ISS(2);
      accl = mfma16(ones, pf, accl);
      PVM(0); PVM(1);
      ISS(3);
      PVM(2); PVM(3);
      ISS(4);
      PVM(4); PVM(5);
      ISS(5);
      PVM(6); PVM(7);
      ISS(6);
      PACE_STEP();
      ISS(7);
      PACE_TAKE();
      src += 32768;
      fs = cs; cs = (cs == RS - 1) ? 0 : cs + 1;
    }
#pragma nounroll
    for (int i = 33 - RS; i < 32; ++i) {
      const LAS char* base = wring + cs * 8192;
      const f32x4 sc = *(const LAS f32x4*)(scw + i * 128);
      v2u pf = PFRAG(sc);
      accl = mfma16(ones, pf, accl);
      asm volatile("" : "+v"(pf), "+v"(accl));
      if (i == 31) waitvm<0>(); else waitvm<8>();
#pragma unroll
      for (int p = 0; p < 8; ++p) {
        const LAS char* a = base + (vro ^ (p << 5));
        acc[p] = mfma16(ldtr(a), pf, acc[p]);
        acc[p + 8] = mfma16(ldtr(a + 256), pf, acc[p + 8]);
      }
      cs = (cs == RS - 1) ? 0 : cs + 1;
    }
    waitlgkm();
    if (c < 2) {
#pragma unroll
      for (int d = 0; d < 16; ++d) *(LAS f32x4*)(lds + L_RING + wave * (RS * 8192) + c * 1024 + (16 * d + 4 * g) * 4) = acc[d];
      if (g == 0) *(LAS f32x2*)(lds + L_ST + wave * 16 + c * 8) = f32x2{M, accl[0]};
    }
    STAMP(3);
    bar();
    const v2u tag = *(const LAS v2u*)(lds + L_TAG);
    {
      float Mx, Ls, o0, o1;
      merge4(lds + L_ST + hd * 8, lds + L_RING + hd * 1024 + wave * 256 + (lane & 31) * 8, RS * 8192, Mx, Ls, o0, o1);
      if (wave != m) {
        const __amdgpu_buffer_rsrc_t ro = RSRC(ws + W_INB + (size_t)(grp * 4 + wave) * 8192 + 4096 + (((m - wave) & 3) - 1) * 1152, 1152);
        __builtin_amdgcn_raw_buffer_store_b128(v4u{asu(o0), tag.x, asu(o1), tag.y}, ro, lane * 16, 0, SC1);
        if ((lane & 31) == 0) __builtin_amdgcn_raw_buffer_store_b128(v4u{asu(Mx), tag.x, asu(Ls), tag.y}, ro, 1024 + hd * 16, 0, SC1);
        STAMP(4);
        const __amdgpu_buffer_rsrc_t ri = RSRC(ws + W_INB + (size_t)bid * 8192 + 4096 + (((wave - m) & 3) - 1) * 1152, 1152);
        v4u v, s;
        for (unsigned sp = 0; sp < SPIN_CAP; ++sp) {
          v = poll128(ri, lane * 16);
          s = poll128(ri, 1024 + hd * 16);
          if (__all((v.y == tag.x) & (v.w == tag.y) & (s.y == tag.x) & (s.w == tag.y))) break;
        }
        o0 = asf(v.x); o1 = asf(v.z); Mx = asf(s.x); Ls = asf(s.z);
      }
      *(LAS f32x2*)(lds + L_RX + wave * 512 + lane * 8) = f32x2{o0, o1};
      if ((lane & 31) == 0) *(LAS f32x2*)(lds + L_RS + wave * 16 + hd * 8) = f32x2{Mx, Ls};
    }
    bar();
    if (wave == 0 && jb < 28) {
      const __amdgpu_buffer_rsrc_t rs = RSRC(ws + W_INB + (size_t)bid * 8192, 4096);
      v4u v[4];
      __builtin_amdgcn_s_sleep(FSLEEP);
      for (unsigned sp = 0; sp < SPIN_CAP; ++sp) {
#pragma unroll
        for (int it = 0; it < 4; ++it) v[it] = poll128(rs, it * 1024 + lane * 16);
        bool ok = true;
#pragma unroll
        for (int it = 0; it < 4; ++it) ok &= (v[it].y == tag.x) & (v[it].w == tag.y);
        if (__all(ok)) break;
      }
      float s0 = (asf(v[0].x) + asf(v[1].x)) + (asf(v[2].x) + asf(v[3].x));
      float s1 = (asf(v[0].z) + asf(v[1].z)) + (asf(v[2].z) + asf(v[3].z));
      s0 += asf(__builtin_amdgcn_mov_dpp((int)asu(s0), 0x128, 0xf, 0xf, true));
      s1 += asf(__builtin_amdgcn_mov_dpp((int)asu(s1), 0x128, 0xf, 0xf, true));
      s0 = rows_sum(s0); s1 = rows_sum(s1);
      if (lane < 8) out[b * (DM / 2) + jb * 8 + lane] = cvtpk(s0, s1);
      STAMP(5);
    }
  } else {
    const int hw = wave - 4;
    if (hw == 0) {
      const int cb = (bid & 7) * 32 + (bid >> 3);
      const __amdgpu_buffer_rsrc_t rq = RSRC(ws + W_QG, 131072);
      const int qin = b * 16384 + kvh * 2048 + lane * 16, qout = (c & 7) * 16384 + (cb * 4 + g) * 16;
      const v4u o0 = poll128(rq, qin), o1 = poll128(rq, qin + 1024), mine = poll128(rq, qout);
      const unsigned l0 = (unsigned)(size_t)lds + L_SC;
      const unsigned voff = (lane >> 1) * 8192 + (lane & 1) * 16;
#pragma unroll
      for (int s = 0; s < 14; ++s) { set_m0(l0 + s * 1024); dma1(voff, wq + (size_t)s * 32 * 8192 + cb * 32); }
      v4u xf[14];
#pragma unroll
      for (int s = 0; s < 14; ++s) xf[s] = *(const GAS v4u*)(x + (c & 7) * (DM * 2) + s * 64 + g * 16);
      waitvm<0>();
      STAMP(6);
      f32x4 qa = {0.f, 0.f, 0.f, 0.f}, qb = {0.f, 0.f, 0.f, 0.f};
      const LAS char* lp = lds + L_SC + (8 * g + ((lane >> 2) & 3)) * 32 + (lane & 3) * 8;
#pragma unroll
      for (int s = 0; s < 14; s += 2) {
        const v2u a0 = ldtr(lp + s * 1024), a1 = ldtr(lp + s * 1024 + 128), b0 = ldtr(lp + s * 1024 + 1024), b1 = ldtr(lp + s * 1024 + 1152);
        qa = mfma32(v4u{a0.x, a0.y, a1.x, a1.y}, xf[s], qa);
        qb = mfma32(v4u{b0.x, b0.y, b1.x, b1.y}, xf[s + 1], qb);
      }
      qa += qb;
      if (c < 8) __builtin_amdgcn_raw_buffer_store_b128(v4u{cvtpk(qa[0], qa[1]), mine.y + 1u, cvtpk(qa[2], qa[3]), mine.w + 1u}, rq, qout, 0, SC1);
      v4u v0, v1;
      bool got = false;
      for (unsigned sp = 0; sp < QSPIN; ++sp) {
        __builtin_amdgcn_s_sleep(QSLEEP);
        v0 = poll128(rq, qin);
        v1 = poll128(rq, qin + 1024);
        if (__all((v0.y != o0.y) & (v0.w != o0.w) & (v1.y != o1.y) & (v1.w != o1.w))) { got = true; break; }
      }
      if (got) {
        *(LAS v2u*)(lds + L_Q + lane * 8) = v2u{v0.x, v0.z};
        *(LAS v2u*)(lds + L_Q + 512 + lane * 8) = v2u{v1.x, v1.z};
      } else {
        float a8[8];
#pragma unroll
        for (int e = 0; e < 8; ++e) a8[e] = 0.f;
        const char* wp = wq + kvh * 1024 + lane * 16;
#pragma unroll 16
        for (int k = 0; k < DM; ++k) {
          const v4u w = *(const GAS v4u*)(wp + (size_t)k * 8192);
          const float xk = asf((unsigned)*(const GAS unsigned short*)(x + (b * DM + k) * 2) << 16);
#pragma unroll
          for (int e = 0; e < 4; ++e) { a8[2 * e] = fmaf(xk, asf(w[e] << 16), a8[2 * e]); a8[2 * e + 1] = fmaf(xk, asf(w[e] & 0xffff0000u), a8[2 * e + 1]); }
        }
        *(LAS v4u*)(lds + L_Q + lane * 16) = v4u{cvtpk(a8[0], a8[1]), cvtpk(a8[2], a8[3]), cvtpk(a8[4], a8[5]), cvtpk(a8[6], a8[7])};
      }
      STAMP(2);
    }
    bar();
    if (hw == 1) {
      const unsigned long long clk = __builtin_amdgcn_s_memrealtime();
      const unsigned n0 = (unsigned)clk, n1 = (unsigned)(clk >> 8) & ~1u;
      const __amdgpu_buffer_rsrc_t rr = RSRC(ws + W_REQ + (size_t)bid * 256, 256);
      if (lane == 0) __builtin_amdgcn_raw_buffer_store_b64(v2u{n0, n1}, rr, 0, 0, SC1);
      v4u rep = {0u, 0u, 0u, 0u};
      for (unsigned sp = 0; sp < SPIN_CAP; ++sp) {
        __builtin_amdgcn_s_sleep(64);
        rep = poll128(rr, 128);
        if (__builtin_amdgcn_readfirstlane(rep.y) == n0 && __builtin_amdgcn_readfirstlane(rep.w) == n1) break;
      }
      if (lane == 0) {
        __builtin_amdgcn_raw_buffer_store_b64(v2u{rep.x, rep.z}, rr, 0, 0, SC1);
        *(LAS v2u*)(lds + L_TAG) = v2u{rep.x, rep.z};
      }
      STAMP(2);
    } else if (hw == 2 && jb == 0) {
      const unsigned long long clk = __builtin_amdgcn_s_memrealtime();
      const unsigned t0 = (unsigned)clk, t1 = (unsigned)(clk >> 8) | 1u;
      const __amdgpu_buffer_rsrc_t rq = RSRC(ws + W_REQ + (size_t)bid * 256, 32 * 256);
      v2u last = {t0, t1};
      for (unsigned sp = 0; sp < SPIN_CAP; ++sp) {
        __builtin_amdgcn_s_sleep(32);
        const v2u r = poll64(rq, (lane & 31) * 256);
        const bool d = (r.x == t0) & (r.y == t1);
        const bool plaus = (sp >= 64) | (r.x - t0 + 0x100000u < 0x200000u);
        if (lane < 32 && !d && plaus && (r.x != last.x || r.y != last.y)) {
          __builtin_amdgcn_raw_buffer_store_b128(v4u{t0, r.x, t1, r.y}, rq, lane * 256 + 128, 0, SC1);
          last = r;
        }
        if (__all(d)) break;
      }
    }
    const int ch = c < 14 ? c : 13;
    const char* wp = wo + (size_t)((2 * kvh + (g >> 1)) * 256 + 64 * m + 32 * (g & 1)) * (DM * 2) + hw * 224 + ch * 16;
    unsigned wr[16][8];
#pragma unroll
    for (int i0 = 0; i0 < 16; i0 += WOB) {
      v4u r[2 * WOB];
#pragma unroll
      for (int u = 0; u < 2 * WOB; ++u) r[u] = *(const GAS v4u*)(wp + (size_t)(2 * i0 + u) * (DM * 2));
#pragma unroll
      for (int u = 0; u < WOB; ++u)
#pragma unroll
        for (int e = 0; e < 4; ++e) {
          wr[i0 + u][2 * e] = __builtin_amdgcn_perm(r[2 * u + 1][e], r[2 * u][e], 0x05040100u);
          wr[i0 + u][2 * e + 1] = __builtin_amdgcn_perm(r[2 * u + 1][e], r[2 * u][e], 0x07060302u);
        }
#pragma unroll
      for (int u = 0; u < WOB; ++u)
        asm volatile("" : "+v"(wr[i0 + u][0]), "+v"(wr[i0 + u][1]), "+v"(wr[i0 + u][2]), "+v"(wr[i0 + u][3]), "+v"(wr[i0 + u][4]), "+v"(wr[i0 + u][5]), "+v"(wr[i0 + u][6]), "+v"(wr[i0 + u][7])::"memory");
    }
    STAMP(1);
    bar();
    const v2u tag = *(const LAS v2u*)(lds + L_TAG);
    STAMP(3);
    bar();
    STAMP(4);
    float Mx, Ls, o0, o1;
    merge4(lds + L_RS + hd * 8, lds + L_RX + lane * 8, 512, Mx, Ls, o0, o1);
    const float inv = __builtin_amdgcn_rcpf(Ls);
    const unsigned op = cvtpk(o0 * inv, o1 * inv);
    float y[8];
#pragma unroll
    for (int e = 0; e < 8; ++e) y[e] = 0.f;
#define STEP(i) { const unsigned ob = row_bcast<i>(op); _Pragma("unroll") for (int e = 0; e < 8; ++e) y[e] = dot2(wr[i][e], ob, y[e]); }
    STEP(0) STEP(1) STEP(2) STEP(3) STEP(4) STEP(5) STEP(6) STEP(7) STEP(8) STEP(9) STEP(10) STEP(11) STEP(12) STEP(13) STEP(14) STEP(15)
#undef STEP
    float z[4];
#pragma unroll
    for (int e = 0; e < 4; ++e) { auto s = __builtin_amdgcn_permlane32_swap(asu(y[e]), asu(y[e + 4]), false, false); z[e] = asf(s[0]) + asf(s[1]); }
    float u[2];
#pragma unroll
    for (int e = 0; e < 2; ++e) { auto s = __builtin_amdgcn_permlane16_swap(asu(z[e]), asu(z[e + 2]), false, false); u[e] = asf(s[0]) + asf(s[1]); }
    if (c < 14) {
      const int n0 = hw * 112 + c * 8 + g * 2;
      const __amdgpu_buffer_rsrc_t ri = RSRC(ws + W_INB + (size_t)b * (32 * 8192), 32 * 8192);
      __builtin_amdgcn_raw_buffer_store_b128(v4u{asu(u[0]), tag.x, asu(u[1]), tag.y}, ri, (n0 >> 4) * 8192 + jb * 128 + (n0 & 15) * 8, 0, SC1);
    }
    STAMP(5);
  }
  STAMP_FLUSH();
}

extern "C" void kernel_launch(void* const* d_in, const int* in_sizes, int n_in, void* d_out, int out_size, void* d_ws, size_t ws_size, hipStream_t stream) {
  (void)hipFuncSetAttribute((const void*)k_main, hipFuncAttributeMaxDynamicSharedMemorySize, L_END);
  k_main<<<256, 512, L_END, stream>>>((const char*)d_in[0], (const char*)d_in[1], (const char*)d_in[2], (const char*)d_in[3], (const char*)d_in[4], (char*)d_ws, (unsigned*)d_out);
}
```

```cpp
#define PRET2
#define PG 8
#define TAU 1080
#define DUTY 8
#include <hip/hip_runtime.h>
#include <hip/hip_bf16.h>
#include <type_traits>
#define WODEF
#define WQDEF

typedef unsigned v4u __attribute__((ext_vector_type(4)));
typedef unsigned v2u __attribute__((ext_vector_type(2)));
typedef float f32x4 __attribute__((ext_vector_type(4)));
typedef float f32x2 __attribute__((ext_vector_type(2)));
typedef short bf16x8 __attribute__((ext_vector_type(8)));
typedef short s16x4 __attribute__((ext_vector_type(4)));
typedef __bf16 bf16x2 __attribute__((ext_vector_type(2)));
#define LAS __attribute__((address_space(3)))
#define GAS __attribute__((address_space(1)))

#define RS 4
#define RH 4
#define WLO 4
#define WMID 6
#define WHI 8
#define DUTY0 8
#define TOFF 0
#define QSLEEP 8
#define QSPIN 64
#define WOB 2
#define WOT0 1800
#define CROUNDS 4
#define CT0 2000
#define CSTEP 1200
#define CLAG 250
#define FSLEEP 12

static_assert(RH >= 1 && RH <= RS && RH <= 4, "parked tiles come from ring slots 0..RH-1");
constexpr int DM = 448, SK = 8192, DH = 256;
constexpr float C2 = 0.0625f * 1.4426950408889634f;
constexpr unsigned SPIN_CAP = 1u << 15;

constexpr size_t W_REQ = 0;
constexpr size_t W_QG = 65536;
constexpr size_t W_INB = W_QG + 131072;
constexpr size_t W_CK = W_INB + 256 * 8192;

constexpr int L_RING = 0;
constexpr int L_SC = 4 * RS * 8192;
constexpr int L_Q = L_SC + 16384;
constexpr int L_ST = L_Q + 1024;
constexpr int L_RX = L_ST + 64;
constexpr int L_RS = L_RX + 2048;
constexpr int L_TAG = L_RS + 64;
constexpr int L_END = L_TAG + 64;
#define TRC(s_) do {} while (0)

template <int N> __device__ __forceinline__ void waitvm() { asm volatile("s_waitcnt vmcnt(%0)" ::"i"(N) : "memory"); }
__device__ __forceinline__ void waitlgkm() { asm volatile("s_waitcnt lgkmcnt(0)" ::: "memory"); }
__device__ __forceinline__ void bar() { asm volatile("s_waitcnt lgkmcnt(0)\n\ts_barrier" ::: "memory"); }
__device__ __forceinline__ void set_m0(unsigned v) { asm volatile("s_mov_b32 m0, %0\n\ts_nop 0" ::"s"(v) : "memory"); }
__device__ __forceinline__ void dma1(unsigned voff, const void* sbase) {
  asm volatile("global_load_lds_dwordx4 %0, %1" ::"v"(voff), "s"(sbase) : "memory");
}
#define BR3(p) ((((p) & 1) << 2) | ((p) & 2) | (((p) >> 2) & 1))
#define WMASK(d) ((1u << (d)) - 1u)
template <int P, int OFF> __device__ __forceinline__ void piece(unsigned wm, unsigned voff, const void* sbase) {
  asm volatile(
      "s_waitcnt vmcnt(%5)\n\t"
      "s_bitcmp1_b32 %0, %7\n\t"
      "s_cbranch_scc1 1f\n\t"
      "s_waitcnt vmcnt(%4)\n\t"
      "s_bitcmp1_b32 %0, %6\n\t"
      "s_cbranch_scc1 1f\n\t"
      "s_waitcnt vmcnt(%3)\n"
      "1:\n\t"
      "global_load_lds_dwordx4 %1, %2 offset:%8 nt" ::"s"(wm), "v"(voff), "s"(sbase), "i"(WLO - 1), "i"(WMID - 1), "i"(WHI - 1), "i"(BR3(P)), "i"(BR3(P) + 8), "i"(OFF)
      : "memory", "scc");
}
__device__ __forceinline__ f32x4 mfma32(v4u a, v4u b, f32x4 c) {
  return __builtin_amdgcn_mfma_f32_16x16x32_bf16(__builtin_bit_cast(bf16x8, a), __builtin_bit_cast(bf16x8, b), c, 0, 0, 0);
}
__device__ __forceinline__ f32x4 mfma16(v2u a, v2u b, f32x4 c) {
  return __builtin_amdgcn_mfma_f32_16x16x16bf16_1k(__builtin_bit_cast(s16x4, a), __builtin_bit_cast(s16x4, b), c, 0, 0, 0);
}
__device__ __forceinline__ v2u ldtr(const LAS char* p) { return __builtin_bit_cast(v2u, __builtin_amdgcn_ds_read_tr16_b64_v4i16((LAS s16x4*)p)); }
__device__ __forceinline__ unsigned cvtpk(float lo, float hi) { return __builtin_bit_cast(unsigned, __builtin_convertvector(f32x2{lo, hi}, bf16x2)); }
__device__ __forceinline__ float dot2(unsigned a, unsigned b, float acc) {
  return __builtin_amdgcn_fdot2_f32_bf16(__builtin_bit_cast(bf16x2, a), __builtin_bit_cast(bf16x2, b), acc, false);
}
__device__ __forceinline__ float asf(unsigned u) { return __builtin_bit_cast(float, u); }
__device__ __forceinline__ unsigned asu(float f) { return __builtin_bit_cast(unsigned, f); }
__device__ __forceinline__ float ex2(float x) { return __builtin_amdgcn_exp2f(x); }
__device__ __forceinline__ float rows_max(float x) {
  auto s = __builtin_amdgcn_permlane16_swap(asu(x), asu(x), false, false);
  x = fmaxf(asf(s[0]), asf(s[1]));
  auto t = __builtin_amdgcn_permlane32_swap(asu(x), asu(x), false, false);
  return fmaxf(asf(t[0]), asf(t[1]));
}
__device__ __forceinline__ float rows_sum(float x) {
  auto s = __builtin_amdgcn_permlane16_swap(asu(x), asu(x), false, false);
  x = asf(s[0]) + asf(s[1]);
  auto t = __builtin_amdgcn_permlane32_swap(asu(x), asu(x), false, false);
  return asf(t[0]) + asf(t[1]);
}
template <int N> __device__ __forceinline__ unsigned row_bcast(unsigned x) { return (unsigned)__builtin_amdgcn_mov_dpp((int)x, 0x150 + N, 0xf, 0xf, true); }
#define RSRC(p, n) __builtin_amdgcn_make_buffer_rsrc((void*)(p), 0, (int)(n), 0x00020000)
constexpr int SC1 = 16;
__device__ __forceinline__ v4u poll128(__amdgpu_buffer_rsrc_t rs, int off) {
  asm volatile("" : "+v"(off)::"memory");
  return __builtin_amdgcn_raw_buffer_load_b128(rs, off, 0, SC1);
}
__device__ __forceinline__ v2u poll64(__amdgpu_buffer_rsrc_t rs, int off) {
  asm volatile("" : "+v"(off)::"memory");
  return __builtin_amdgcn_raw_buffer_load_b64(rs, off, 0, SC1);
}
#define PINV(x) asm volatile("" : "+v"(x))
#define STAMP(i) do {} while (0)
#define STAMP_FLUSH() do {} while (0)

__device__ __forceinline__ void merge4(const LAS char* stp, const LAS char* pap, int pstride, float& Mx, float& Ls, float& o0, float& o1) {
  f32x2 st[4], pa[4];
#pragma unroll
  for (int w = 0; w < 4; ++w) {
    st[w] = *(const LAS f32x2*)(stp + w * 16);
    pa[w] = *(const LAS f32x2*)(pap + w * pstride);
  }
  Mx = fmaxf(fmaxf(st[0].x, st[1].x), fmaxf(st[2].x, st[3].x));
  Ls = 0.f; o0 = 0.f; o1 = 0.f;
#pragma unroll
  for (int w = 0; w < 4; ++w) {
    const float f = ex2(st[w].x - Mx);
    Ls = fmaf(f, st[w].y, Ls); o0 = fmaf(f, pa[w].x, o0); o1 = fmaf(f, pa[w].y, o1);
  }
}

__global__ void __launch_bounds__(512, 2) k_main(const char* __restrict__ x, const char* __restrict__ Kc, const char* __restrict__ Vc, const char* __restrict__ wq,
                                                 const char* __restrict__ wo, char* ws, unsigned* out) {
  extern __shared__ __attribute__((aligned(16))) char lds_[];
  LAS char* lds = (LAS char*)lds_;
  const int tid = threadIdx.x, lane = tid & 63, wave = __builtin_amdgcn_readfirstlane(tid >> 6);
  const int bid = blockIdx.x, grp = bid >> 2, m = bid & 3, b = bid >> 5, jb = bid & 31, kvh = jb >> 2;
  const int g = lane >> 4, c = lane & 15, hd = lane >> 5;
  STAMP(0);
  volatile LAS unsigned* qflag = (volatile LAS unsigned*)(lds + L_TAG + 8);
  if (tid < 6) qflag[tid] = 0u;
  bar();
  if (__builtin_expect(wave < 4, 1)) {
    __builtin_amdgcn_s_setprio(3);
    const size_t t0 = (size_t)bid * 1048576 + wave * 8192;
    const char* src = Kc + t0;
    const unsigned ring0 = (unsigned)(size_t)lds + L_RING + wave * (RS * 8192);
    unsigned vo[8];
#pragma unroll
    for (int p = 0; p < 8; ++p) vo[p] = hd * 512 + ((((lane & 31) ^ hd) * 16) ^ (p << 5)) + (p >> 2) * 4096;
    unsigned wm = WMASK(DUTY0);
    unsigned sched = (unsigned)__builtin_amdgcn_s_memrealtime() * 10u + (unsigned)(TOFF * 10 + (RH + RS) * TAU);
    unsigned tnow = 0, adj = 0;
#define ISS(p) do { if (((p) & 3) == 0) set_m0(dst + ((p) >> 2) * 4096); piece<(p), ((p) & 3) * 1024>(wm, vo[p], src); } while (0)
#define PACE_STEP() do { tnow = (unsigned)__builtin_amdgcn_s_memrealtime(); adj = qflag[1]; } while (0)
#define PACE_TAKE() do { const int late_ = (int)(tnow * 10u - sched); if (lane == 0) qflag[2 + wave] = (unsigned)late_; \
      int d_ = DUTY + (((late_ - (int)adj) * PG) / 1280); d_ = d_ < 0 ? 0 : d_ > 16 ? 16 : d_; wm = __builtin_amdgcn_readfirstlane(WMASK(d_)); sched += TAU; } while (0)
    const LAS char* wring = lds + L_RING + wave * (RS * 8192);
    LAS char* scw = lds + L_SC + wave * 4096 + (g * 2 + (c & 1)) * 16;
    const int kro = c * 512 + ((g ^ c) * 16);
#define KFRAG(base_, p) (*(const LAS v4u*)((base_) + ((kro ^ (((p) & 3) << 6)) + ((p) >> 2) * 256)))
    v4u hold[RH][8];
#pragma nounroll
    for (int r = 0;; ++r) {
      const int sl = r >= RS ? r - RS : r;
      TRC(r);
      if (r >= RS) {
        waitvm<8 * (RS - 1)>();
#pragma unroll
        for (int j = 0; j < RH; ++j)
          if (sl == j) {
#pragma unroll
            for (int p = 0; p < 8; ++p) hold[j][p] = KFRAG(wring + j * 8192, p);
          }
        waitlgkm();
        if (r == RS + RH - 1) break;
      }
      const unsigned dst = ring0 + sl * 8192;
      ISS(0); ISS(1); ISS(2); ISS(3); ISS(4); ISS(5); ISS(6); ISS(7);
      src += 32768;
    }
    STAMP(6);
    for (unsigned sp = 0; sp < (1u << 22) && *qflag == 0u; ++sp) __builtin_amdgcn_s_sleep(1);
    asm volatile("" ::: "memory");
    STAMP(1);
    v4u qf[8];
    float mx = -INFINITY;
    f32x4 acc[16], accl;
    int fs = RH - 1, cs = RH % RS;
#define HOLD(r) do { if (FIRST && (r) < RH) { f32x4 h = {0.f, 0.f, 0.f, 0.f}; _Pragma("unroll") for (int p = 0; p < 8; ++p) h = mfma32(hold[(r) < RH ? (r) : 0][p], qf[p], h); \
      if (c < 2) *(LAS f32x4*)(scw + (r) * 128) = h; mx = fmaxf(fmaxf(mx, h[0]), fmaxf(fmaxf(h[1], h[2]), h[3])); PINV(mx); } } while (0)
    auto step_a = [&](auto first, int s) {
      constexpr bool FIRST = decltype(first)::value;
      waitvm<8 * (RS - 2)>();
      const LAS char* base = wring + cs * 8192;
      const unsigned dst = ring0 + fs * 8192;
      ISS(0);
      if (FIRST) {
#pragma unroll
        for (int p = 0; p < 8; ++p) qf[p] = *(const LAS v4u*)(lds + L_Q + (c & 1) * 512 + p * 64 + g * 16);
      }
      v4u kf[8];
#pragma unroll
      for (int p = 0; p < 8; ++p) kf[p] = KFRAG(base, p);
      TRC(s + RS - 1);
      f32x4 a = {0.f, 0.f, 0.f, 0.f};
      ISS(1);
      a = mfma32(kf[0], qf[0], a); a = mfma32(kf[1], qf[1], a); PINV(a);
      ISS(2);
      a = mfma32(kf[2], qf[2], a); a = mfma32(kf[3], qf[3], a); PINV(a);
      HOLD(0);
      ISS(3);
      a = mfma32(kf[4], qf[4], a); a = mfma32(kf[5], qf[5], a); PINV(a);
      HOLD(1);
      ISS(4);
      a = mfma32(kf[6], qf[6], a); a = mfma32(kf[7], qf[7], a); PINV(a);
      HOLD(2);
      ISS(5);
      if (c < 2) *(LAS f32x4*)(scw + s * 128) = a;
      mx = fmaxf(fmaxf(mx, a[0]), fmaxf(fmaxf(a[1], a[2]), a[3])); PINV(mx);
      HOLD(3);
      ISS(6);
      if (FIRST) {
        accl = f32x4{0.f, 0.f, 0.f, 0.f}; PINV(accl);
#pragma unroll
        for (int d = 0; d < 16; ++d) { acc[d] = f32x4{0.f, 0.f, 0.f, 0.f}; PINV(acc[d]); }
      }
      PACE_STEP();
      ISS(7);
      PACE_TAKE();
      src += 32768;
      fs = cs; cs = (cs == RS - 1) ? 0 : cs + 1;
    };
    step_a(std::true_type{}, RH);
#pragma nounroll
    for (int s = RH + 1; s < 32; ++s) {
      if (s == 33 - RS) {
        src = Vc + t0;
#pragma unroll
        for (int p = 0; p < 8; ++p) vo[p] = hd * 512 + ((((lane & 31) ^ (hd << 1)) * 16) ^ ((p & 3) << 6)) + (p >> 2) * 4096;
      }
      step_a(std::false_type{}, s);
    }
    const float M = rows_max(mx) * C2;
    STAMP(2);
    const int kq = 4 * g + ((lane >> 2) & 3);
    const int vro = kq * 512 + (kq & 7) * 32 + (lane & 3) * 8;
    const v2u ones = {0x3F803F80u, 0x3F803F80u};
#define PFRAG(sc_) v2u{cvtpk(ex2(fmaf(sc_[0], C2, -M)), ex2(fmaf(sc_[1], C2, -M))), cvtpk(ex2(fmaf(sc_[2], C2, -M)), ex2(fmaf(sc_[3], C2, -M)))}
#define PVM(p) do { acc[p] = mfma16(vf[p], pf, acc[p]); acc[(p) + 8] = mfma16(vf[(p) + 8], pf, acc[(p) + 8]); asm volatile("" : "+v"(acc[p]), "+v"(acc[(p) + 8])); } while (0)
#pragma nounroll
    for (int i = 0; i < 33 - RS; ++i) {
      waitvm<8 * (RS - 2)>();
      const LAS char* base = wring + cs * 8192;
      const unsigned dst = ring0 + fs * 8192;
      ISS(0);
      const f32x4 sc = *(const LAS f32x4*)(scw + i * 128);
      v2u vf[16];
#pragma unroll
      for (int p = 0; p < 4; ++p) { const LAS char* a = base + (vro ^ (p << 5)); vf[p] = ldtr(a); vf[p + 8] = ldtr(a + 256); }
      TRC(32 + i + RS - 1);
      ISS(1);
#pragma unroll
      for (int p = 4; p < 8; ++p) { const LAS char* a = base + (vro ^ (p << 5)); vf[p] = ldtr(a); vf[p + 8] = ldtr(a + 256); }
      v2u pf = PFRAG(sc);
      PINV(pf);
      ISS(2);
      accl = mfma16(ones, pf, accl);
      PVM(0); PVM(1);
      ISS(3);
      PVM(2); PVM(3);
      ISS(4);
      PVM(4); PVM(5);
      ISS(5);
      PVM(6); PVM(7);
      ISS(6);
      PACE_STEP();
      ISS(7);
      PACE_TAKE();
      src += 32768;
      fs = cs; cs = (cs == RS - 1) ? 0 : cs + 1;
    }
#pragma nounroll
    for (int i = 33 - RS; i < 32; ++i) {
      const LAS char* base = wring + cs * 8192;
      const f32x4 sc = *(const LAS f32x4*)(scw + i * 128);
      v2u pf = PFRAG(sc);
      accl = mfma16(ones, pf, accl);
      asm volatile("" : "+v"(pf), "+v"(accl));
      if (i == 31) waitvm<0>(); else waitvm<8>();
#pragma unroll
      for (int p = 0; p < 8; ++p) {
        const LAS char* a = base + (vro ^ (p << 5));
        acc[p] = mfma16(ldtr(a), pf, acc[p]);
        acc[p + 8] = mfma16(ldtr(a + 256), pf, acc[p + 8]);
      }
      cs = (cs == RS - 1) ? 0 : cs + 1;
    }
    waitlgkm();
    if (c < 2) {
#pragma unroll
      for (int d = 0; d < 16; ++d) *(LAS f32x4*)(lds + L_RING + wave * (RS * 8192) + c * 1024 + (16 * d + 4 * g) * 4) = acc[d];
      if (g == 0) *(LAS f32x2*)(lds + L_ST + wave * 16 + c * 8) = f32x2{M, accl[0]};
    }
    STAMP(3);
    bar();
    const v2u tag = *(const LAS v2u*)(lds + L_TAG);
    {
      float Mx, Ls, o0, o1;
      merge4(lds + L_ST + hd * 8, lds + L_RING + hd * 1024 + wave * 256 + (lane & 31) * 8, RS * 8192, Mx, Ls, o0, o1);
      if (wave != m) {
        const __amdgpu_buffer_rsrc_t ro = RSRC(ws + W_INB + (size_t)(grp * 4 + wave) * 8192 + 4096 + (((m - wave) & 3) - 1) * 1152, 1152);
        __builtin_amdgcn_raw_buffer_store_b128(v4u{asu(o0), tag.x, asu(o1), tag.y}, ro, lane * 16, 0, SC1);
        if ((lane & 31) == 0) __builtin_amdgcn_raw_buffer_store_b128(v4u{asu(Mx), tag.x, asu(Ls), tag.y}, ro, 1024 + hd * 16, 0, SC1);
        STAMP(4);
        const __amdgpu_buffer_rsrc_t ri = RSRC(ws + W_INB + (size_t)bid * 8192 + 4096 + (((wave - m) & 3) - 1) * 1152, 1152);
        v4u v, s;
        for (unsigned sp = 0; sp < SPIN_CAP; ++sp) {
          v = poll128(ri, lane * 16);
          s = poll128(ri, 1024 + hd * 16);
          if (__all((v.y == tag.x) & (v.w == tag.y) & (s.y == tag.x) & (s.w == tag.y))) break;
        }
        o0 = asf(v.x); o1 = asf(v.z); Mx = asf(s.x); Ls = asf(s.z);
      }
      else {
        v2u u = poll64(RSRC(ws + W_INB + (size_t)bid * 8192, 4096), lane * 64);
        asm volatile("" ::"v"(u));
      }
      *(LAS f32x2*)(lds + L_RX + wave * 512 + lane * 8) = f32x2{o0, o1};
      if ((lane & 31) == 0) *(LAS f32x2*)(lds + L_RS + wave * 16 + hd * 8) = f32x2{Mx, Ls};
    }
    bar();
    if (wave == 0 && jb < 28) {
      const __amdgpu_buffer_rsrc_t rs = RSRC(ws + W_INB + (size_t)bid * 8192, 4096);
      v4u v[4];
      __builtin_amdgcn_s_sleep(FSLEEP);
      for (unsigned sp = 0; sp < SPIN_CAP; ++sp) {
#pragma unroll
        for (int it = 0; it < 4; ++it) v[it] = poll128(rs, it * 1024 + lane * 16);
        bool ok = true;
#pragma unroll
        for (int it = 0; it < 4; ++it) ok &= (v[it].y == tag.x) & (v[it].w == tag.y);
        if (__all(ok)) break;
      }
      float s0 = (asf(v[0].x) + asf(v[1].x)) + (asf(v[2].x) + asf(v[3].x));
      float s1 = (asf(v[0].z) + asf(v[1].z)) + (asf(v[2].z) + asf(v[3].z));
      s0 += asf(__builtin_amdgcn_mov_dpp((int)asu(s0), 0x128, 0xf, 0xf, true));
      s1 += asf(__builtin_amdgcn_mov_dpp((int)asu(s1), 0x128, 0xf, 0xf, true));
      s0 = rows_sum(s0); s1 = rows_sum(s1);
      if (lane < 8) out[b * (DM / 2) + jb * 8 + lane] = cvtpk(s0, s1);
      STAMP(5);
    }
  } else {
    const int hw = wave - 4;
    const unsigned tk0 = (unsigned)__builtin_amdgcn_s_memrealtime();
    if (hw == 0) {
      const int cb = (bid & 7) * 32 + (bid >> 3);
      const __amdgpu_buffer_rsrc_t rq = RSRC(ws + W_QG, 131072);
      const int qin = b * 16384 + kvh * 2048 + lane * 16, qout = (c & 7) * 16384 + (cb * 4 + g) * 16;
      const v4u o0 = poll128(rq, qin), o1 = poll128(rq, qin + 1024), mine = poll128(rq, qout);
      const unsigned l0 = (unsigned)(size_t)lds + L_SC;
      const unsigned voff = (lane >> 1) * 8192 + (lane & 1) * 16;
#pragma unroll
      for (int s = 0; s < 14; ++s) { set_m0(l0 + s * 1024); dma1(voff, wq + (size_t)s * 32 * 8192 + cb * 32); }
      v4u xf[14];
#pragma unroll
      for (int s = 0; s < 14; ++s) xf[s] = *(const GAS v4u*)(x + (c & 7) * (DM * 2) + s * 64 + g * 16);
      waitvm<0>();
      STAMP(6);
      f32x4 qa = {0.f, 0.f, 0.f, 0.f}, qb = {0.f, 0.f, 0.f, 0.f};
      const LAS char* lp = lds + L_SC + (8 * g + ((lane >> 2) & 3)) * 32 + (lane & 3) * 8;
#pragma unroll
      for (int s = 0; s < 14; s += 2) {
        const v2u a0 = ldtr(lp + s * 1024), a1 = ldtr(lp + s * 1024 + 128), b0 = ldtr(lp + s * 1024 + 1024), b1 = ldtr(lp + s * 1024 + 1152);
        qa = mfma32(v4u{a0.x, a0.y, a1.x, a1.y}, xf[s], qa);
        qb = mfma32(v4u{b0.x, b0.y, b1.x, b1.y}, xf[s + 1], qb);
      }
      qa += qb;
      if (c < 8) __builtin_amdgcn_raw_buffer_store_b128(v4u{cvtpk(qa[0], qa[1]), mine.y + 1u, cvtpk(qa[2], qa[3]), mine.w + 1u}, rq, qout, 0, SC1);
      v4u v0, v1;
      bool got = false;
      for (unsigned sp = 0; sp < QSPIN; ++sp) {
        __builtin_amdgcn_s_sleep(QSLEEP);
        v0 = poll128(rq, qin);
        v1 = poll128(rq, qin + 1024);
        if (__all((v0.y != o0.y) & (v0.w != o0.w) & (v1.y != o1.y) & (v1.w != o1.w))) { got = true; break; }
      }
      if (got) {
        *(LAS v2u*)(lds + L_Q + lane * 8) = v2u{v0.x, v0.z};
        *(LAS v2u*)(lds + L_Q + 512 + lane * 8) = v2u{v1.x, v1.z};
      } else {
        float a8[8];
#pragma unroll
        for (int e = 0; e < 8; ++e) a8[e] = 0.f;
        const char* wp = wq + kvh * 1024 + lane * 16;
#pragma unroll 16
        for (int k = 0; k < DM; ++k) {
          const v4u w = *(const GAS v4u*)(wp + (size_t)k * 8192);
          const float xk = asf((unsigned)*(const GAS unsigned short*)(x + (b * DM + k) * 2) << 16);
#pragma unroll
          for (int e = 0; e < 4; ++e) { a8[2 * e] = fmaf(xk, asf(w[e] << 16), a8[2 * e]); a8[2 * e + 1] = fmaf(xk, asf(w[e] & 0xffff0000u), a8[2 * e + 1]); }
        }
        *(LAS v4u*)(lds + L_Q + lane * 16) = v4u{cvtpk(a8[0], a8[1]), cvtpk(a8[2], a8[3]), cvtpk(a8[4], a8[5]), cvtpk(a8[6], a8[7])};
      }
      waitlgkm();
      if (lane == 0) *qflag = 1u;
      STAMP(2);
    } else {
      for (unsigned sp = 0; sp < (1u << 22) && *qflag == 0u; ++sp) __builtin_amdgcn_s_sleep(16);
    }
    if (hw == 1) {
      const unsigned long long clk = __builtin_amdgcn_s_memrealtime();
      const unsigned n0 = (unsigned)clk, n1 = (unsigned)(clk >> 8) & ~1u;
      const __amdgpu_buffer_rsrc_t rr = RSRC(ws + W_REQ + (size_t)bid * 256, 256);
      if (lane == 0) __builtin_amdgcn_raw_buffer_store_b64(v2u{n0, n1}, rr, 0, 0, SC1);
      v4u rep = {0u, 0u, 0u, 0u};
      for (unsigned sp = 0; sp < SPIN_CAP; ++sp) {
        __builtin_amdgcn_s_sleep(64);
        rep = poll128(rr, 128);
        if (__builtin_amdgcn_readfirstlane(rep.y) == n0 && __builtin_amdgcn_readfirstlane(rep.w) == n1) break;
      }
      if (lane == 0) {
        __builtin_amdgcn_raw_buffer_store_b64(v2u{rep.x, rep.z}, rr, 0, 0, SC1);
        *(LAS v2u*)(lds + L_TAG) = v2u{rep.x, rep.z};
      }
      STAMP(2);
    } else if (hw == 2 && jb == 0) {
      const unsigned long long clk = __builtin_amdgcn_s_memrealtime();
      const unsigned t0 = (unsigned)clk, t1 = (unsigned)(clk >> 8) | 1u;
      const __amdgpu_buffer_rsrc_t rq = RSRC(ws + W_REQ + (size_t)bid * 256, 32 * 256);
      v2u last = {t0, t1};
      for (unsigned sp = 0; sp < SPIN_CAP; ++sp) {
        __builtin_amdgcn_s_sleep(32);
        const v2u r = poll64(rq, (lane & 31) * 256);
        const bool d = (r.x == t0) & (r.y == t1);
        const bool plaus = (sp >= 64) | (r.x - t0 + 0x100000u < 0x200000u);
        if (lane < 32 && !d && plaus && (r.x != last.x || r.y != last.y)) {
          __builtin_amdgcn_raw_buffer_store_b128(v4u{t0, r.x, t1, r.y}, rq, lane * 256 + 128, 0, SC1);
          last = r;
        }
        if (__all(d)) break;
      }
    }
    const int ch = c < 14 ? c : 13;
    const char* wp = wo + (size_t)((2 * kvh + (g >> 1)) * 256 + 64 * m + 32 * (g & 1)) * (DM * 2) + hw * 224 + ch * 16;
    unsigned wr[16][8];
#pragma unroll
    for (int i0 = 0; i0 < 16; i0 += WOB) {
      v4u r[2 * WOB];
#pragma unroll
      for (int u = 0; u < 2 * WOB; ++u) r[u] = *(const GAS v4u*)(wp + (size_t)(2 * i0 + u) * (DM * 2));
#pragma unroll
      for (int u = 0; u < WOB; ++u)
#pragma unroll
        for (int e = 0; e < 4; ++e) {
          wr[i0 + u][2 * e] = __builtin_amdgcn_perm(r[2 * u + 1][e], r[2 * u][e], 0x05040100u);
          wr[i0 + u][2 * e + 1] = __builtin_amdgcn_perm(r[2 * u + 1][e], r[2 * u][e], 0x07060302u);
        }
#pragma unroll
      for (int u = 0; u < WOB; ++u)
        asm volatile("" : "+v"(wr[i0 + u][0]), "+v"(wr[i0 + u][1]), "+v"(wr[i0 + u][2]), "+v"(wr[i0 + u][3]), "+v"(wr[i0 + u][4]), "+v"(wr[i0 + u][5]), "+v"(wr[i0 + u][6]), "+v"(wr[i0 + u][7])::"memory");
    }
    if (hw == 3) {
      const __amdgpu_buffer_rsrc_t rc = RSRC(ws + W_CK, 64 * 4352);
      for (int k = 0; k < CROUNDS; ++k) {
        const unsigned tgo = tk0 + CT0 + k * CSTEP;
        for (unsigned sp = 0; sp < SPIN_CAP && (int)((unsigned)__builtin_amdgcn_s_memrealtime() - tgo) < 0; ++sp) __builtin_amdgcn_s_sleep(16);
        if (bid % 17 == k && lane < 4) {
          const int l4 = ((int)qflag[2] + (int)qflag[3] + (int)qflag[4] + (int)qflag[5]) >> 2;
          __builtin_amdgcn_raw_buffer_store_b64(v2u{(unsigned)l4, tgo}, rc, (k * 4 + lane) * 4352 + (bid / 17) * 8, 0, SC1);
        }
        for (unsigned sp = 0; sp < SPIN_CAP && (int)((unsigned)__builtin_amdgcn_s_memrealtime() - tgo) < CLAG; ++sp) __builtin_amdgcn_s_sleep(16);
        const v2u r = poll64(rc, (k * 4 + ((bid >> 3) & 3)) * 4352 + c * 8);
        const bool ok = (r.y - tgo + 500u < 1000u) & ((int)r.x > -20000) & ((int)r.x < 20000);
        float sum = ok ? (float)(int)r.x : 0.f, cnt = ok ? 1.f : 0.f;
#pragma unroll
        for (int o = 1; o < 16; o <<= 1) { sum += __shfl_xor(sum, o); cnt += __shfl_xor(cnt, o); }
        if (cnt >= 8.f && lane == 0) qflag[1] = (unsigned)(int)(sum / cnt);
      }
    }
    STAMP(1);
    bar();
    const v2u tag = *(const LAS v2u*)(lds + L_TAG);
    STAMP(3);
    bar();
    STAMP(4);
    float Mx, Ls, o0, o1;
    merge4(lds + L_RS + hd * 8, lds + L_RX + lane * 8, 512, Mx, Ls, o0, o1);
    const float inv = __builtin_amdgcn_rcpf(Ls);
    const unsigned op = cvtpk(o0 * inv, o1 * inv);
    float y[8];
#pragma unroll
    for (int e = 0; e < 8; ++e) y[e] = 0.f;
#define STEP(i) { const unsigned ob = row_bcast<i>(op); _Pragma("unroll") for (int e = 0; e < 8; ++e) y[e] = dot2(wr[i][e], ob, y[e]); }
    STEP(0) STEP(1) STEP(2) STEP(3) STEP(4) STEP(5) STEP(6) STEP(7) STEP(8) STEP(9) STEP(10) STEP(11) STEP(12) STEP(13) STEP(14) STEP(15)
#undef STEP
    float z[4];
#pragma unroll
    for (int e = 0; e < 4; ++e) { auto s = __builtin_amdgcn_permlane32_swap(asu(y[e]), asu(y[e + 4]), false, false); z[e] = asf(s[0]) + asf(s[1]); }
    float u[2];
#pragma unroll
    for (int e = 0; e < 2; ++e) { auto s = __builtin_amdgcn_permlane16_swap(asu(z[e]), asu(z[e + 2]), false, false); u[e] = asf(s[0]) + asf(s[1]); }
    if (c < 14) {
      const int n0 = hw * 112 + c * 8 + g * 2;
      const __amdgpu_buffer_rsrc_t ri = RSRC(ws + W_INB + (size_t)b * (32 * 8192), 32 * 8192);
      __builtin_amdgcn_raw_buffer_store_b128(v4u{asu(u[0]), tag.x, asu(u[1]), tag.y}, ri, (n0 >> 4) * 8192 + jb * 128 + (n0 & 15) * 8, 0, SC1);
    }
    STAMP(5);
  }
  STAMP_FLUSH();
}

extern "C" void kernel_launch(void* const* d_in, const int* in_sizes, int n_in, void* d_out, int out_size, void* d_ws, size_t ws_size, hipStream_t stream) {
  (void)hipFuncSetAttribute((const void*)k_main, hipFuncAttributeMaxDynamicSharedMemorySize, L_END);
  k_main<<<256, 512, L_END, stream>>>((const char*)d_in[0], (const char*)d_in[1], (const char*)d_in[2], (const char*)d_in[3], (const char*)d_in[4], (char*)d_ws, (unsigned*)d_out);
}
```

```cpp
#define PRET2
#define PG 8
#define TAU 1080
#define DUTY 8
#define STAMPS
#include <hip/hip_runtime.h>
#include <hip/hip_bf16.h>
#include <type_traits>
#define WODEF
#define WQDEF

typedef unsigned v4u __attribute__((ext_vector_type(4)));
typedef unsigned v2u __attribute__((ext_vector_type(2)));
typedef float f32x4 __attribute__((ext_vector_type(4)));
typedef float f32x2 __attribute__((ext_vector_type(2)));
typedef short bf16x8 __attribute__((ext_vector_type(8)));
typedef short s16x4 __attribute__((ext_vector_type(4)));
typedef __bf16 bf16x2 __attribute__((ext_vector_type(2)));
#define LAS __attribute__((address_space(3)))
#define GAS __attribute__((address_space(1)))

#define RS 4
#define RH 4
#define WLO 4
#define WMID 6
#define WHI 8
#define DUTY0 8
#define TOFF 0
#define QSLEEP 8
#define QSPIN 64
#define WOB 2
#define WOT0 1800
#define CROUNDS 4
#define CT0 2000
#define CSTEP 1200
#define CLAG 250
#define FSLEEP 12

static_assert(RH >= 1 && RH <= RS && RH <= 4, "parked tiles come from ring slots 0..RH-1");
constexpr int DM = 448, SK = 8192, DH = 256;
constexpr float C2 = 0.0625f * 1.4426950408889634f;
constexpr unsigned SPIN_CAP = 1u << 15;

constexpr size_t W_REQ = 0;
constexpr size_t W_QG = 65536;
constexpr size_t W_INB = W_QG + 131072;
constexpr size_t W_CK = W_INB + 256 * 8192;
constexpr size_t W_TS = 8u << 20;

constexpr int L_RING = 0;
constexpr int L_SC = 4 * RS * 8192;
constexpr int L_Q = L_SC + 16384;
constexpr int L_ST = L_Q + 1024;
constexpr int L_RX = L_ST + 64;
constexpr int L_RS = L_RX + 2048;
constexpr int L_TAG = L_RS + 64;
constexpr int L_END = L_TAG + 64;
#define TRC(s_) do {} while (0)

template <int N> __device__ __forceinline__ void waitvm() { asm volatile("s_waitcnt vmcnt(%0)" ::"i"(N) : "memory"); }
__device__ __forceinline__ void waitlgkm() { asm volatile("s_waitcnt lgkmcnt(0)" ::: "memory"); }
__device__ __forceinline__ void bar() { asm volatile("s_waitcnt lgkmcnt(0)\n\ts_barrier" ::: "memory"); }
__device__ __forceinline__ void set_m0(unsigned v) { asm volatile("s_mov_b32 m0, %0\n\ts_nop 0" ::"s"(v) : "memory"); }
__device__ __forceinline__ void dma1(unsigned voff, const void* sbase) {
  asm volatile("global_load_lds_dwordx4 %0, %1" ::"v"(voff), "s"(sbase) : "memory");
}
#define BR3(p) ((((p) & 1) << 2) | ((p) & 2) | (((p) >> 2) & 1))
#define WMASK(d) ((1u << (d)) - 1u)
template <int P, int OFF> __device__ __forceinline__ void piece(unsigned wm, unsigned voff, const void* sbase) {
  asm volatile(
      "s_waitcnt vmcnt(%5)\n\t"
      "s_bitcmp1_b32 %0, %7\n\t"
      "s_cbranch_scc1 1f\n\t"
      "s_waitcnt vmcnt(%4)\n\t"
      "s_bitcmp1_b32 %0, %6\n\t"
      "s_cbranch_scc1 1f\n\t"
      "s_waitcnt vmcnt(%3)\n"
      "1:\n\t"
      "global_load_lds_dwordx4 %1, %2 offset:%8 nt" ::"s"(wm), "v"(voff), "s"(sbase), "i"(WLO - 1), "i"(WMID - 1), "i"(WHI - 1), "i"(BR3(P)), "i"(BR3(P) + 8), "i"(OFF)
      : "memory", "scc");
}
__device__ __forceinline__ f32x4 mfma32(v4u a, v4u b, f32x4 c) {
  return __builtin_amdgcn_mfma_f32_16x16x32_bf16(__builtin_bit_cast(bf16x8, a), __builtin_bit_cast(bf16x8, b), c, 0, 0, 0);
}
__device__ __forceinline__ f32x4 mfma16(v2u a, v2u b, f32x4 c) {
  return __builtin_amdgcn_mfma_f32_16x16x16bf16_1k(__builtin_bit_cast(s16x4, a), __builtin_bit_cast(s16x4, b), c, 0, 0, 0);
}
__device__ __forceinline__ v2u ldtr(const LAS char* p) { return __builtin_bit_cast(v2u, __builtin_amdgcn_ds_read_tr16_b64_v4i16((LAS s16x4*)p)); }
__device__ __forceinline__ unsigned cvtpk(float lo, float hi) { return __builtin_bit_cast(unsigned, __builtin_convertvector(f32x2{lo, hi}, bf16x2)); }
__device__ __forceinline__ float dot2(unsigned a, unsigned b, float acc) {
  return __builtin_amdgcn_fdot2_f32_bf16(__builtin_bit_cast(bf16x2, a), __builtin_bit_cast(bf16x2, b), acc, false);
}
__device__ __forceinline__ float asf(unsigned u) { return __builtin_bit_cast(float, u); }
__device__ __forceinline__ unsigned asu(float f) { return __builtin_bit_cast(unsigned, f); }
__device__ __forceinline__ float ex2(float x) { return __builtin_amdgcn_exp2f(x); }
__device__ __forceinline__ float rows_max(float x) {
  auto s = __builtin_amdgcn_permlane16_swap(asu(x), asu(x), false, false);
  x = fmaxf(asf(s[0]), asf(s[1]));
  auto t = __builtin_amdgcn_permlane32_swap(asu(x), asu(x), false, false);
  return fmaxf(asf(t[0]), asf(t[1]));
}
__device__ __forceinline__ float rows_sum(float x) {
  auto s = __builtin_amdgcn_permlane16_swap(asu(x), asu(x), false, false);
  x = asf(s[0]) + asf(s[1]);
  auto t = __builtin_amdgcn_permlane32_swap(asu(x), asu(x), false, false);
  return asf(t[0]) + asf(t[1]);
}
template <int N> __device__ __forceinline__ unsigned row_bcast(unsigned x) { return (unsigned)__builtin_amdgcn_mov_dpp((int)x, 0x150 + N, 0xf, 0xf, true); }
#define RSRC(p, n) __builtin_amdgcn_make_buffer_rsrc((void*)(p), 0, (int)(n), 0x00020000)
constexpr int SC1 = 16;
__device__ __forceinline__ v4u poll128(__amdgpu_buffer_rsrc_t rs, int off) {
  asm volatile("" : "+v"(off)::"memory");
  return __builtin_amdgcn_raw_buffer_load_b128(rs, off, 0, SC1);
}
__device__ __forceinline__ v2u poll64(__amdgpu_buffer_rsrc_t rs, int off) {
  asm volatile("" : "+v"(off)::"memory");
  return __builtin_amdgcn_raw_buffer_load_b64(rs, off, 0, SC1);
}
#define PINV(x) asm volatile("" : "+v"(x))
#define STAMP(i) do { tsv[i] = __builtin_amdgcn_s_memrealtime(); asm volatile("s_waitcnt lgkmcnt(0)" : "+s"(tsv[i]) :: "memory"); } while (0)
#define STAMP_FLUSH() do { if (lane == 0) { _Pragma("unroll") for (int i_ = 0; i_ < 8; ++i_) ((unsigned long long*)(ws + W_TS))[(blockIdx.x * 8 + wave) * 8 + i_] = tsv[i_]; } } while (0)

__device__ __forceinline__ void merge4(const LAS char* stp, const LAS char* pap, int pstride, float& Mx, float& Ls, float& o0, float& o1) {
  f32x2 st[4], pa[4];
#pragma unroll
  for (int w = 0; w < 4; ++w) {
    st[w] = *(const LAS f32x2*)(stp + w * 16);
    pa[w] = *(const LAS f32x2*)(pap + w * pstride);
  }
  Mx = fmaxf(fmaxf(st[0].x, st[1].x), fmaxf(st[2].x, st[3].x));
  Ls = 0.f; o0 = 0.f; o1 = 0.f;
#pragma unroll
  for (int w = 0; w < 4; ++w) {
    const float f = ex2(st[w].x - Mx);
    Ls = fmaf(f, st[w].y, Ls); o0 = fmaf(f, pa[w].x, o0); o1 = fmaf(f, pa[w].y, o1);
  }
}

__global__ void __launch_bounds__(512, 2) k_main(const char* __restrict__ x, const char* __restrict__ Kc, const char* __restrict__ Vc, const char* __restrict__ wq,
                                                 const char* __restrict__ wo, char* ws, unsigned* out) {
  extern __shared__ __attribute__((aligned(16))) char lds_[];
  LAS char* lds = (LAS char*)lds_;
  const int tid = threadIdx.x, lane = tid & 63, wave = __builtin_amdgcn_readfirstlane(tid >> 6);
  const int bid = blockIdx.x, grp = bid >> 2, m = bid & 3, b = bid >> 5, jb = bid & 31, kvh = jb >> 2;
  const int g = lane >> 4, c = lane & 15, hd = lane >> 5;
  unsigned long long tsv[8] = {0, 0, 0, 0, 0, 0, 0, 0};
  STAMP(0);
  volatile LAS unsigned* qflag = (volatile LAS unsigned*)(lds + L_TAG + 8);
  if (tid < 6) qflag[tid] = 0u;
  bar();
  if (__builtin_expect(wave < 4, 1)) {
    __builtin_amdgcn_s_setprio(3);
    const size_t t0 = (size_t)bid * 1048576 + wave * 8192;
    const char* src = Kc + t0;
    const unsigned ring0 = (unsigned)(size_t)lds + L_RING + wave * (RS * 8192);
    unsigned vo[8];
#pragma unroll
    for (int p = 0; p < 8; ++p) vo[p] = hd * 512 + ((((lane & 31) ^ hd) * 16) ^ (p << 5)) + (p >> 2) * 4096;
    unsigned wm = WMASK(DUTY0);
    unsigned sched = (unsigned)__builtin_amdgcn_s_memrealtime() * 10u + (unsigned)(TOFF * 10 + (RH + RS) * TAU);
    unsigned tnow = 0, adj = 0;
#define ISS(p) do { if (((p) & 3) == 0) set_m0(dst + ((p) >> 2) * 4096); piece<(p), ((p) & 3) * 1024>(wm, vo[p], src); } while (0)
#define PACE_STEP() do { tnow = (unsigned)__builtin_amdgcn_s_memrealtime(); adj = qflag[1]; } while (0)
#define PACE_TAKE() do { const int late_ = (int)(tnow * 10u - sched); if (lane == 0) qflag[2 + wave] = (unsigned)late_; \
      int d_ = DUTY + (((late_ - (int)adj) * PG) / 1280); d_ = d_ < 0 ? 0 : d_ > 16 ? 16 : d_; wm = __builtin_amdgcn_readfirstlane(WMASK(d_)); sched += TAU; } while (0)
    const LAS char* wring = lds + L_RING + wave * (RS * 8192);
    LAS char* scw = lds + L_SC + wave * 4096 + (g * 2 + (c & 1)) * 16;
    const int kro = c * 512 + ((g ^ c) * 16);
#define KFRAG(base_, p) (*(const LAS v4u*)((base_) + ((kro ^ (((p) & 3) << 6)) + ((p) >> 2) * 256)))
    v4u hold[RH][8];
#pragma nounroll
    for (int r = 0;; ++r) {
      const int sl = r >= RS ? r - RS : r;
      TRC(r);
      if (r >= RS) {
        waitvm<8 * (RS - 1)>();
#pragma unroll
        for (int j = 0; j < RH; ++j)
          if (sl == j) {
#pragma unroll
            for (int p = 0; p < 8; ++p) hold[j][p] = KFRAG(wring + j * 8192, p);
          }
        waitlgkm();
        if (r == RS + RH - 1) break;
      }
      const unsigned dst = ring0 + sl * 8192;
      ISS(0); ISS(1); ISS(2); ISS(3); ISS(4); ISS(5); ISS(6); ISS(7);
      src += 32768;
    }
    STAMP(6);
    for (unsigned sp = 0; sp < (1u << 22) && *qflag == 0u; ++sp) __builtin_amdgcn_s_sleep(1);
    asm volatile("" ::: "memory");
    STAMP(1);
    v4u qf[8];
    float mx = -INFINITY;
    f32x4 acc[16], accl;
    int fs = RH - 1, cs = RH % RS;
#define HOLD(r) do { if (FIRST && (r) < RH) { f32x4 h = {0.f, 0.f, 0.f, 0.f}; _Pragma("unroll") for (int p = 0; p < 8; ++p) h = mfma32(hold[(r) < RH ? (r) : 0][p], qf[p], h); \
      if (c < 2) *(LAS f32x4*)(scw + (r) * 128) = h; mx = fmaxf(fmaxf(mx, h[0]), fmaxf(fmaxf(h[1], h[2]), h[3])); PINV(mx); } } while (0)
    auto step_a = [&](auto first, int s) {
      constexpr bool FIRST = decltype(first)::value;
      waitvm<8 * (RS - 2)>();
      const LAS char* base = wring + cs * 8192;
      const unsigned dst = ring0 + fs * 8192;
      ISS(0);
      if (FIRST) {
#pragma unroll
        for (int p = 0; p < 8; ++p) qf[p] = *(const LAS v4u*)(lds + L_Q + (c & 1) * 512 + p * 64 + g * 16);
      }
      v4u kf[8];
#pragma unroll
      for (int p = 0; p < 8; ++p) kf[p] = KFRAG(base, p);
      TRC(s + RS - 1);
      f32x4 a = {0.f, 0.f, 0.f, 0.f};
      ISS(1);
      a = mfma32(kf[0], qf[0], a); a = mfma32(kf[1], qf[1], a); PINV(a);
      ISS(2);
      a = mfma32(kf[2], qf[2], a); a = mfma32(kf[3], qf[3], a); PINV(a);
      HOLD(0);
      ISS(3);
      a = mfma32(kf[4], qf[4], a); a = mfma32(kf[5], qf[5], a); PINV(a);
      HOLD(1);
      ISS(4);
      a = mfma32(kf[6], qf[6], a); a = mfma32(kf[7], qf[7], a); PINV(a);
      HOLD(2);
      ISS(5);
      if (c < 2) *(LAS f32x4*)(scw + s * 128) = a;
      mx = fmaxf(fmaxf(mx, a[0]), fmaxf(fmaxf(a[1], a[2]), a[3])); PINV(mx);
      HOLD(3);
      ISS(6);
      if (FIRST) {
        accl = f32x4{0.f, 0.f, 0.f, 0.f}; PINV(accl);
#pragma unroll
        for (int d = 0; d < 16; ++d) { acc[d] = f32x4{0.f, 0.f, 0.f, 0.f}; PINV(acc[d]); }
      }
      PACE_STEP();
      ISS(7);
      PACE_TAKE();
      src += 32768;
      fs = cs; cs = (cs == RS - 1) ? 0 : cs + 1;
    };
    step_a(std::true_type{}, RH);
#pragma nounroll
    for (int s = RH + 1; s < 32; ++s) {
      if (s == 33 - RS) {
        src = Vc + t0;
#pragma unroll
        for (int p = 0; p < 8; ++p) vo[p] = hd * 512 + ((((lane & 31) ^ (hd << 1)) * 16) ^ ((p & 3) << 6)) + (p >> 2) * 4096;
      }
      step_a(std::false_type{}, s);
    }
    const float M = rows_max(mx) * C2;
    STAMP(2);
    const int kq = 4 * g + ((lane >> 2) & 3);
    const int vro = kq * 512 + (kq & 7) * 32 + (lane & 3) * 8;
    const v2u ones = {0x3F803F80u, 0x3F803F80u};
#define PFRAG(sc_) v2u{cvtpk(ex2(fmaf(sc_[0], C2, -M)), ex2(fmaf(sc_[1], C2, -M))), cvtpk(ex2(fmaf(sc_[2], C2, -M)), ex2(fmaf(sc_[3], C2, -M)))}
#define PVM(p) do { acc[p] = mfma16(vf[p], pf, acc[p]); acc[(p) + 8] = mfma16(vf[(p) + 8], pf, acc[(p) + 8]); asm volatile("" : "+v"(acc[p]), "+v"(acc[(p) + 8])); } while (0)
#pragma nounroll
    for (int i = 0; i < 33 - RS; ++i) {
      waitvm<8 * (RS - 2)>();
      const LAS char* base = wring + cs * 8192;
      const unsigned dst = ring0 + fs * 8192;
      ISS(0);
      const f32x4 sc = *(const LAS f32x4*)(scw + i * 128);
      v2u vf[16];
#pragma unroll
      for (int p = 0; p < 4; ++p) { const LAS char* a = base + (vro ^ (p << 5)); vf[p] = ldtr(a); vf[p + 8] = ldtr(a + 256); }
      TRC(32 + i + RS - 1);
      ISS(1);
#pragma unroll
      for (int p = 4; p < 8; ++p) { const LAS char* a = base + (vro ^ (p << 5)); vf[p] = ldtr(a); vf[p + 8] = ldtr(a + 256); }
      v2u pf = PFRAG(sc);
      PINV(pf);
      ISS(2);
      accl = mfma16(ones, pf, accl);
      PVM(0); PVM(1);
      ISS(3);
      PVM(2); PVM(3);
      ISS(4);
      PVM(4); PVM(5);
      ISS(5);
      PVM(6); PVM(7);
      ISS(6);
      PACE_STEP();
      ISS(7);
      PACE_TAKE();
      src += 32768;
      fs = cs; cs = (cs == RS - 1) ? 0 : cs + 1;
    }
#pragma nounroll
    for (int i = 33 - RS; i < 32; ++i) {
      const LAS char* base = wring + cs * 8192;
      const f32x4 sc = *(const LAS f32x4*)(scw + i * 128);
      v2u pf = PFRAG(sc);
      accl = mfma16(ones, pf, accl);
      asm volatile("" : "+v"(pf), "+v"(accl));
      if (i == 31) waitvm<0>(); else waitvm<8>();
#pragma unroll
      for (int p = 0; p < 8; ++p) {
        const LAS char* a = base + (vro ^ (p << 5));
        acc[p] = mfma16(ldtr(a), pf, acc[p]);
        acc[p + 8] = mfma16(ldtr(a + 256), pf, acc[p + 8]);
      }
      cs = (cs == RS - 1) ? 0 : cs + 1;
    }
    waitlgkm();
    if (c < 2) {
#pragma unroll
      for (int d = 0; d < 16; ++d) *(LAS f32x4*)(lds + L_RING + wave * (RS * 8192) + c * 1024 + (16 * d + 4 * g) * 4) = acc[d];
      if (g == 0) *(LAS f32x2*)(lds + L_ST + wave * 16 + c * 8) = f32x2{M, accl[0]};
    }
    STAMP(3);
    bar();
    const v2u tag = *(const LAS v2u*)(lds + L_TAG);
    {
      float Mx, Ls, o0, o1;
      merge4(lds + L_ST + hd * 8, lds + L_RING + hd * 1024 + wave * 256 + (lane & 31) * 8, RS * 8192, Mx, Ls, o0, o1);
      if (wave != m) {
        const __amdgpu_buffer_rsrc_t ro = RSRC(ws + W_INB + (size_t)(grp * 4 + wave) * 8192 + 4096 + (((m - wave) & 3) - 1) * 1152, 1152);
        __builtin_amdgcn_raw_buffer_store_b128(v4u{asu(o0), tag.x, asu(o1), tag.y}, ro, lane * 16, 0, SC1);
        if ((lane & 31) == 0) __builtin_amdgcn_raw_buffer_store_b128(v4u{asu(Mx), tag.x, asu(Ls), tag.y}, ro, 1024 + hd * 16, 0, SC1);
        STAMP(4);
        const __amdgpu_buffer_rsrc_t ri = RSRC(ws + W_INB + (size_t)bid * 8192 + 4096 + (((wave - m) & 3) - 1) * 1152, 1152);
        v4u v, s;
        for (unsigned sp = 0; sp < SPIN_CAP; ++sp) {
          v = poll128(ri, lane * 16);
          s = poll128(ri, 1024 + hd * 16);
          if (__all((v.y == tag.x) & (v.w == tag.y) & (s.y == tag.x) & (s.w == tag.y))) break;
        }
        o0 = asf(v.x); o1 = asf(v.z); Mx = asf(s.x); Ls = asf(s.z);
      }
      else {
        v2u u = poll64(RSRC(ws + W_INB + (size_t)bid * 8192, 4096), lane * 64);
        asm volatile("" ::"v"(u));
      }
      *(LAS f32x2*)(lds + L_RX + wave * 512 + lane * 8) = f32x2{o0, o1};
      if ((lane & 31) == 0) *(LAS f32x2*)(lds + L_RS + wave * 16 + hd * 8) = f32x2{Mx, Ls};
    }
    bar();
    if (wave == 0 && jb < 28) {
      const __amdgpu_buffer_rsrc_t rs = RSRC(ws + W_INB + (size_t)bid * 8192, 4096);
      v4u v[4];
      __builtin_amdgcn_s_sleep(FSLEEP);
      for (unsigned sp = 0; sp < SPIN_CAP; ++sp) {
#pragma unroll
        for (int it = 0; it < 4; ++it) v[it] = poll128(rs, it * 1024 + lane * 16);
        bool ok = true;
#pragma unroll
        for (int it = 0; it < 4; ++it) ok &= (v[it].y == tag.x) & (v[it].w == tag.y);
        if (__all(ok)) break;
      }
      float s0 = (asf(v[0].x) + asf(v[1].x)) + (asf(v[2].x) + asf(v[3].x));
      float s1 = (asf(v[0].z) + asf(v[1].z)) + (asf(v[2].z) + asf(v[3].z));
      s0 += asf(__builtin_amdgcn_mov_dpp((int)asu(s0), 0x128, 0xf, 0xf, true));
      s1 += asf(__builtin_amdgcn_mov_dpp((int)asu(s1), 0x128, 0xf, 0xf, true));
      s0 = rows_sum(s0); s1 = rows_sum(s1);
      if (lane < 8) out[b * (DM / 2) + jb * 8 + lane] = cvtpk(s0, s1);
      STAMP(5);
    }
  } else {
    const int hw = wave - 4;
    const unsigned tk0 = (unsigned)__builtin_amdgcn_s_memrealtime();
    if (hw == 0) {
      const int cb = (bid & 7) * 32 + (bid >> 3);
      const __amdgpu_buffer_rsrc_t rq = RSRC(ws + W_QG, 131072);
      const int qin = b * 16384 + kvh * 2048 + lane * 16, qout = (c & 7) * 16384 + (cb * 4 + g) * 16;
      const v4u o0 = poll128(rq, qin), o1 = poll128(rq, qin + 1024), mine = poll128(rq, qout);
      const unsigned l0 = (unsigned)(size_t)lds + L_SC;
      const unsigned voff = (lane >> 1) * 8192 + (lane & 1) * 16;
#pragma unroll
      for (int s = 0; s < 14; ++s) { set_m0(l0 + s * 1024); dma1(voff, wq + (size_t)s * 32 * 8192 + cb * 32); }
      v4u xf[14];
#pragma unroll
      for (int s = 0; s < 14; ++s) xf[s] = *(const GAS v4u*)(x + (c & 7) * (DM * 2) + s * 64 + g * 16);
      waitvm<0>();
      STAMP(6);
      f32x4 qa = {0.f, 0.f, 0.f, 0.f}, qb = {0.f, 0.f, 0.f, 0.f};
      const LAS char* lp = lds + L_SC + (8 * g + ((lane >> 2) & 3)) * 32 + (lane & 3) * 8;
#pragma unroll
      for (int s = 0; s < 14; s += 2) {
        const v2u a0 = ldtr(lp + s * 1024), a1 = ldtr(lp + s * 1024 + 128), b0 = ldtr(lp + s * 1024 + 1024), b1 = ldtr(lp + s * 1024 + 1152);
        qa = mfma32(v4u{a0.x, a0.y, a1.x, a1.y}, xf[s], qa);
        qb = mfma32(v4u{b0.x, b0.y, b1.x, b1.y}, xf[s + 1], qb);
      }
      qa += qb;
      if (c < 8) __builtin_amdgcn_raw_buffer_store_b128(v4u{cvtpk(qa[0], qa[1]), mine.y + 1u, cvtpk(qa[2], qa[3]), mine.w + 1u}, rq, qout, 0, SC1);
      v4u v0, v1;
      bool got = false;
      for (unsigned sp = 0; sp < QSPIN; ++sp) {
        __builtin_amdgcn_s_sleep(QSLEEP);
        v0 = poll128(rq, qin);
        v1 = poll128(rq, qin + 1024);
        if (__all((v0.y != o0.y) & (v0.w != o0.w) & (v1.y != o1.y) & (v1.w != o1.w))) { got = true; break; }
      }
      if (got) {
        *(LAS v2u*)(lds + L_Q + lane * 8) = v2u{v0.x, v0.z};
        *(LAS v2u*)(lds + L_Q + 512 + lane * 8) = v2u{v1.x, v1.z};
      } else {
        float a8[8];
#pragma unroll
        for (int e = 0; e < 8; ++e) a8[e] = 0.f;
        const char* wp = wq + kvh * 1024 + lane * 16;
#pragma unroll 16
        for (int k = 0; k < DM; ++k) {
          const v4u w = *(const GAS v4u*)(wp + (size_t)k * 8192);
          const float xk = asf((unsigned)*(const GAS unsigned short*)(x + (b * DM + k) * 2) << 16);
#pragma unroll
          for (int e = 0; e < 4; ++e) { a8[2 * e] = fmaf(xk, asf(w[e] << 16), a8[2 * e]); a8[2 * e + 1] = fmaf(xk, asf(w[e] & 0xffff0000u), a8[2 * e + 1]); }
        }
        *(LAS v4u*)(lds + L_Q + lane * 16) = v4u{cvtpk(a8[0], a8[1]), cvtpk(a8[2], a8[3]), cvtpk(a8[4], a8[5]), cvtpk(a8[6], a8[7])};
      }
      waitlgkm();
      if (lane == 0) *qflag = 1u;
      STAMP(2);
    } else {
      for (unsigned sp = 0; sp < (1u << 22) && *qflag == 0u; ++sp) __builtin_amdgcn_s_sleep(16);
    }
    if (hw == 1) {
      const unsigned long long clk = __builtin_amdgcn_s_memrealtime();
      const unsigned n0 = (unsigned)clk, n1 = (unsigned)(clk >> 8) & ~1u;
      const __amdgpu_buffer_rsrc_t rr = RSRC(ws + W_REQ + (size_t)bid * 256, 256);
      if (lane == 0) __builtin_amdgcn_raw_buffer_store_b64(v2u{n0, n1}, rr, 0, 0, SC1);
      v4u rep = {0u, 0u, 0u, 0u};
      for (unsigned sp = 0; sp < SPIN_CAP; ++sp) {
        __builtin_amdgcn_s_sleep(64);
        rep = poll128(rr, 128);
        if (__builtin_amdgcn_readfirstlane(rep.y) == n0 && __builtin_amdgcn_readfirstlane(rep.w) == n1) break;
      }
      if (lane == 0) {
        __builtin_amdgcn_raw_buffer_store_b64(v2u{rep.x, rep.z}, rr, 0, 0, SC1);
        *(LAS v2u*)(lds + L_TAG) = v2u{rep.x, rep.z};
      }
      STAMP(2);
    } else if (hw == 2 && jb == 0) {
      const unsigned long long clk = __builtin_amdgcn_s_memrealtime();
      const unsigned t0 = (unsigned)clk, t1 = (unsigned)(clk >> 8) | 1u;
      const __amdgpu_buffer_rsrc_t rq = RSRC(ws + W_REQ + (size_t)bid * 256, 32 * 256);
      v2u last = {t0, t1};
      for (unsigned sp = 0; sp < SPIN_CAP; ++sp) {
        __builtin_amdgcn_s_sleep(32);
        const v2u r = poll64(rq, (lane & 31) * 256);
        const bool d = (r.x == t0) & (r.y == t1);
        const bool plaus = (sp >= 64) | (r.x - t0 + 0x100000u < 0x200000u);
        if (lane < 32 && !d && plaus && (r.x != last.x || r.y != last.y)) {
          __builtin_amdgcn_raw_buffer_store_b128(v4u{t0, r.x, t1, r.y}, rq, lane * 256 + 128, 0, SC1);
          last = r;
        }
        if (__all(d)) break;
      }
    }
    const int ch = c < 14 ? c : 13;
    const char* wp = wo + (size_t)((2 * kvh + (g >> 1)) * 256 + 64 * m + 32 * (g & 1)) * (DM * 2) + hw * 224 + ch * 16;
    unsigned wr[16][8];
#pragma unroll
    for (int i0 = 0; i0 < 16; i0 += WOB) {
      v4u r[2 * WOB];
#pragma unroll
      for (int u = 0; u < 2 * WOB; ++u) r[u] = *(const GAS v4u*)(wp + (size_t)(2 * i0 + u) * (DM * 2));
#pragma unroll
      for (int u = 0; u < WOB; ++u)
#pragma unroll
        for (int e = 0; e < 4; ++e) {
          wr[i0 + u][2 * e] = __builtin_amdgcn_perm(r[2 * u + 1][e], r[2 * u][e], 0x05040100u);
          wr[i0 + u][2 * e + 1] = __builtin_amdgcn_perm(r[2 * u + 1][e], r[2 * u][e], 0x07060302u);
        }
#pragma unroll
      for (int u = 0; u < WOB; ++u)
        asm volatile("" : "+v"(wr[i0 + u][0]), "+v"(wr[i0 + u][1]), "+v"(wr[i0 + u][2]), "+v"(wr[i0 + u][3]), "+v"(wr[i0 + u][4]), "+v"(wr[i0 + u][5]), "+v"(wr[i0 + u][6]), "+v"(wr[i0 + u][7])::"memory");
    }
    if (hw == 3) {
      const __amdgpu_buffer_rsrc_t rc = RSRC(ws + W_CK, 64 * 4352);
      for (int k = 0; k < CROUNDS; ++k) {
        const unsigned tgo = tk0 + CT0 + k * CSTEP;
        for (unsigned sp = 0; sp < SPIN_CAP && (int)((unsigned)__builtin_amdgcn_s_memrealtime() - tgo) < 0; ++sp) __builtin_amdgcn_s_sleep(16);
        if (bid % 17 == k && lane < 4) {
          const int l4 = ((int)qflag[2] + (int)qflag[3] + (int)qflag[4] + (int)qflag[5]) >> 2;
          __builtin_amdgcn_raw_buffer_store_b64(v2u{(unsigned)l4, tgo}, rc, (k * 4 + lane) * 4352 + (bid / 17) * 8, 0, SC1);
        }
        for (unsigned sp = 0; sp < SPIN_CAP && (int)((unsigned)__builtin_amdgcn_s_memrealtime() - tgo) < CLAG; ++sp) __builtin_amdgcn_s_sleep(16);
        const v2u r = poll64(rc, (k * 4 + ((bid >> 3) & 3)) * 4352 + c * 8);
        const bool ok = (r.y - tgo + 500u < 1000u) & ((int)r.x > -20000) & ((int)r.x < 20000);
        float sum = ok ? (float)(int)r.x : 0.f, cnt = ok ? 1.f : 0.f;
#pragma unroll
        for (int o = 1; o < 16; o <<= 1) { sum += __shfl_xor(sum, o); cnt += __shfl_xor(cnt, o); }
        if (cnt >= 8.f && lane == 0) qflag[1] = (unsigned)(int)(sum / cnt);
      }
    }
    STAMP(1);
    bar();
    const v2u tag = *(const LAS v2u*)(lds + L_TAG);
    STAMP(3);
    bar();
    STAMP(4);
    float Mx, Ls, o0, o1;
    merge4(lds + L_RS + hd * 8, lds + L_RX + lane * 8, 512, Mx, Ls, o0, o1);
    const float inv = __builtin_amdgcn_rcpf(Ls);
    const unsigned op = cvtpk(o0 * inv, o1 * inv);
    float y[8];
#pragma unroll
    for (int e = 0; e < 8; ++e) y[e] = 0.f;
#define STEP(i) { const unsigned ob = row_bcast<i>(op); _Pragma("unroll") for (int e = 0; e < 8; ++e) y[e] = dot2(wr[i][e], ob, y[e]); }
    STEP(0) STEP(1) STEP(2) STEP(3) STEP(4) STEP(5) STEP(6) STEP(7) STEP(8) STEP(9) STEP(10) STEP(11) STEP(12) STEP(13) STEP(14) STEP(15)
#undef STEP
    float z[4];
#pragma unroll
    for (int e = 0; e < 4; ++e) { auto s = __builtin_amdgcn_permlane32_swap(asu(y[e]), asu(y[e + 4]), false, false); z[e] = asf(s[0]) + asf(s[1]); }
    float u[2];
#pragma unroll
    for (int e = 0; e < 2; ++e) { auto s = __builtin_amdgcn_permlane16_swap(asu(z[e]), asu(z[e + 2]), false, false); u[e] = asf(s[0]) + asf(s[1]); }
    if (c < 14) {
      const int n0 = hw * 112 + c * 8 + g * 2;
      const __amdgpu_buffer_rsrc_t ri = RSRC(ws + W_INB + (size_t)b * (32 * 8192), 32 * 8192);
      __builtin_amdgcn_raw_buffer_store_b128(v4u{asu(u[0]), tag.x, asu(u[1]), tag.y}, ri, (n0 >> 4) * 8192 + jb * 128 + (n0 & 15) * 8, 0, SC1);
    }
    STAMP(5);
  }
  STAMP_FLUSH();
}

__global__ void k_rep(const char* ws) {
  __shared__ unsigned long long sm[12][256];
  const unsigned long long tr0 = __builtin_amdgcn_s_memrealtime();
  const unsigned long long* ts = (const unsigned long long*)(ws + W_TS);
  const int g = threadIdx.x;
  unsigned long long mn = ~0ull, mx = 0;
  for (int w = 0; w < 8; ++w) { unsigned long long t = ts[(g * 8 + w) * 8]; mn = t < mn ? t : mn; for (int i = 0; i < 8; ++i) { t = ts[(g * 8 + w) * 8 + i]; mx = t > mx ? t : mx; } }
  sm[0][g] = mn; sm[1][g] = mx;
  for (int i = 1; i < 8; ++i) sm[1 + i][g] = ts[(g * 8 + 0) * 8 + i];
  sm[9][g] = ts[(g * 8 + 4) * 8 + 2]; sm[10][g] = ts[(g * 8 + 4) * 8 + 6];
  __syncthreads();
  if (g == 0) {
    for (int i = 0; i < 256; ++i) { mn = sm[0][i] < mn ? sm[0][i] : mn; mx = sm[1][i] > mx ? sm[1][i] : mx; }
    unsigned long long av[12] = {0}, ma[12] = {0};
    for (int r = 2; r < 11; ++r) for (int i = 0; i < 256; ++i) { unsigned long long t = sm[r][i] > mn ? sm[r][i] - mn : 0; av[r] += t; ma[r] = t > ma[r] ? t : ma[r]; }
    unsigned long long D = av[9]/256;
    if (D > 20000) D = 20000;
    const unsigned long long tend = mn + 20000 + D * 20;
    while (__builtin_amdgcn_s_memrealtime() < tend) __builtin_amdgcn_s_sleep(8);
  }
}
extern "C" void kernel_launch(void* const* d_in, const int* in_sizes, int n_in, void* d_out, int out_size, void* d_ws, size_t ws_size, hipStream_t stream) {
  (void)hipFuncSetAttribute((const void*)k_main, hipFuncAttributeMaxDynamicSharedMemorySize, L_END);
  k_main<<<256, 512, L_END, stream>>>((const char*)d_in[0], (const char*)d_in[1], (const char*)d_in[2], (const char*)d_in[3], (const char*)d_in[4], (char*)d_ws, (unsigned*)d_out);
  k_rep<<<1, 256, 0, stream>>>((const char*)d_ws);
}
```

```cpp
#include <hip/hip_runtime.h>
#include <hip/hip_bf16.h>
#include <type_traits>

typedef unsigned v4u __attribute__((ext_vector_type(4)));
typedef unsigned v2u __attribute__((ext_vector_type(2)));
typedef float f32x4 __attribute__((ext_vector_type(4)));
typedef float f32x2 __attribute__((ext_vector_type(2)));
typedef short bf16x8 __attribute__((ext_vector_type(8)));
typedef short s16x4 __attribute__((ext_vector_type(4)));
typedef __bf16 bf16x2 __attribute__((ext_vector_type(2)));
#define LAS __attribute__((address_space(3)))
#define GAS __attribute__((address_space(1)))

constexpr int DM = 448;
constexpr float C2 = 0.0625f * 1.4426950408889634f;
constexpr int RS = 4;
constexpr int RH = 4;
#define WLO 4
#define WMID 6
#define WHI 8
#define DUTY 8
#define TAU 1090
#define TOFF 0
#define PG 8
#define QSPIN 16
#define CROUNDS 4
#define CT0 2000
#define CSTEP 1200
#define CLAG 250
#define CODE_HI 4480
constexpr unsigned SPIN_CAP = 1u << 18, LSPIN_CAP = 1u << 24;

constexpr size_t W_REQ = 0;
constexpr size_t W_QG = 65536;
constexpr size_t W_INB = W_QG + 131072;
constexpr size_t W_CK = W_INB + 256 * 8192;

constexpr int L_RING = 0;
constexpr int L_SC = 4 * RS * 8192;
constexpr int L_Q = L_SC + 16384;
constexpr int L_ST = L_Q + 1024;
constexpr int L_RX = L_ST + 64;
constexpr int L_RS = L_RX + 2048;
constexpr int L_TAG = L_RS + 64;
constexpr int L_X = L_TAG + 64;
constexpr int L_END = L_X + 7168;

template <int N> __device__ __forceinline__ void waitvm() { asm volatile("s_waitcnt vmcnt(%0)" ::"i"(N) : "memory"); }
__device__ __forceinline__ void waitlgkm() { asm volatile("s_waitcnt lgkmcnt(0)" ::: "memory"); }
__device__ __forceinline__ void bar() { asm volatile("s_waitcnt lgkmcnt(0)\n\ts_barrier" ::: "memory"); }
__device__ __forceinline__ unsigned now() { return (unsigned)__builtin_amdgcn_s_memrealtime(); }
__device__ __forceinline__ void set_m0(unsigned v) { asm volatile("s_mov_b32 m0, %0\n\ts_nop 0" ::"s"(v) : "memory"); }
__device__ __forceinline__ void dma1(unsigned voff, const void* sbase) { asm volatile("global_load_lds_dwordx4 %0, %1" ::"v"(voff), "s"(sbase) : "memory"); }
#define BR3(p) ((((p) & 1) << 2) | ((p) & 2) | (((p) >> 2) & 1))
#define WMASK(d) ((1u << (d)) - 1u)
template <bool WAIT, int P, int OFF> __device__ __forceinline__ void piece(unsigned wm, unsigned voff, const void* sbase) {
  if constexpr (WAIT)
    asm volatile(
        "s_waitcnt vmcnt(%5)\n\t"
        "s_bitcmp1_b32 %0, %7\n\t"
        "s_cbranch_scc1 1f\n\t"
        "s_waitcnt vmcnt(%4)\n\t"
        "s_bitcmp1_b32 %0, %6\n\t"
        "s_cbranch_scc1 1f\n\t"
        "s_waitcnt vmcnt(%3)\n"
        "1:\n\t"
        "global_load_lds_dwordx4 %1, %2 offset:%8 nt" ::"s"(wm), "v"(voff), "s"(sbase), "i"(WLO - 1), "i"(WMID - 1), "i"(WHI - 1), "i"(BR3(P)), "i"(BR3(P) + 8), "i"(OFF)
        : "memory", "scc");
  else
    asm volatile("global_load_lds_dwordx4 %0, %1 offset:%2 nt" ::"v"(voff), "s"(sbase), "i"(OFF) : "memory");
}
__device__ __forceinline__ f32x4 mfma32(v4u a, v4u b, f32x4 c) {
  return __builtin_amdgcn_mfma_f32_16x16x32_bf16(__builtin_bit_cast(bf16x8, a), __builtin_bit_cast(bf16x8, b), c, 0, 0, 0);
}
__device__ __forceinline__ f32x4 mfma16(v2u a, v2u b, f32x4 c) {
  return __builtin_amdgcn_mfma_f32_16x16x16bf16_1k(__builtin_bit_cast(s16x4, a), __builtin_bit_cast(s16x4, b), c, 0, 0, 0);
}
__device__ __forceinline__ v2u ldtr(const LAS char* p) { return __builtin_bit_cast(v2u, __builtin_amdgcn_ds_read_tr16_b64_v4i16((LAS s16x4*)p)); }
__device__ __forceinline__ unsigned cvtpk(float lo, float hi) { return __builtin_bit_cast(unsigned, __builtin_convertvector(f32x2{lo, hi}, bf16x2)); }
__device__ __forceinline__ float dot2(unsigned a, unsigned b, float acc) {
  return __builtin_amdgcn_fdot2_f32_bf16(__builtin_bit_cast(bf16x2, a), __builtin_bit_cast(bf16x2, b), acc, false);
}
__device__ __forceinline__ float asf(unsigned u) { return __builtin_bit_cast(float, u); }
__device__ __forceinline__ unsigned asu(float f) { return __builtin_bit_cast(unsigned, f); }
__device__ __forceinline__ float ex2(float x) { return __builtin_amdgcn_exp2f(x); }
__device__ __forceinline__ float rows_max(float x) {
  auto s = __builtin_amdgcn_permlane16_swap(asu(x), asu(x), false, false);
  x = fmaxf(asf(s[0]), asf(s[1]));
  auto t = __builtin_amdgcn_permlane32_swap(asu(x), asu(x), false, false);
  return fmaxf(asf(t[0]), asf(t[1]));
}
__device__ __forceinline__ float rows_sum(float x) {
  auto s = __builtin_amdgcn_permlane16_swap(asu(x), asu(x), false, false);
  x = asf(s[0]) + asf(s[1]);
  auto t = __builtin_amdgcn_permlane32_swap(asu(x), asu(x), false, false);
  return asf(t[0]) + asf(t[1]);
}
template <int N> __device__ __forceinline__ unsigned row_bcast(unsigned x) { return (unsigned)__builtin_amdgcn_mov_dpp((int)x, 0x150 + N, 0xf, 0xf, true); }
#define RSRC(p, n) __builtin_amdgcn_make_buffer_rsrc((void*)(p), 0, (int)(n), 0x00020000)
constexpr int SC1 = 16;
__device__ __forceinline__ v4u poll128(__amdgpu_buffer_rsrc_t rs, int off) {
  asm volatile("" : "+v"(off)::"memory");
  return __builtin_amdgcn_raw_buffer_load_b128(rs, off, 0, SC1);
}
__device__ __forceinline__ v2u poll64(__amdgpu_buffer_rsrc_t rs, int off) {
  asm volatile("" : "+v"(off)::"memory");
  return __builtin_amdgcn_raw_buffer_load_b64(rs, off, 0, SC1);
}
__device__ __forceinline__ void idle_until(unsigned t) {
#pragma nounroll
  for (unsigned sp = 0; sp < 4096u && (int)(now() - t) < 0; ++sp) __builtin_amdgcn_s_sleep(16);
}
#define PINV(x) asm volatile("" : "+v"(x))
#define STAMP(i) do {} while (0)
#define STAMP_FLUSH() do {} while (0)

__device__ __forceinline__ void merge4(const LAS char* stp, const LAS char* pap, int pstride, float& Mx, float& Ls, float& o0, float& o1) {
  f32x2 st[4], pa[4];
#pragma unroll
  for (int w = 0; w < 4; ++w) {
    st[w] = *(const LAS f32x2*)(stp + w * 16);
    pa[w] = *(const LAS f32x2*)(pap + w * pstride);
  }
  Mx = fmaxf(fmaxf(st[0].x, st[1].x), fmaxf(st[2].x, st[3].x));
  Ls = 0.f; o0 = 0.f; o1 = 0.f;
#pragma unroll
  for (int w = 0; w < 4; ++w) {
    const float f = ex2(st[w].x - Mx);
    Ls = fmaf(f, st[w].y, Ls); o0 = fmaf(f, pa[w].x, o0); o1 = fmaf(f, pa[w].y, o1);
  }
}

__global__ void __launch_bounds__(512, 2) k_main(const char* x, const char* Kc, const char* Vc, const char* wq, const char* wo, char* ws, unsigned* out) {
  extern __shared__ __attribute__((aligned(16))) char lds_[];
  LAS char* lds = (LAS char*)lds_;
  const int tid = threadIdx.x, lane = tid & 63, wave = __builtin_amdgcn_readfirstlane(tid >> 6);
  const int bid = blockIdx.x, grp = bid >> 2, m = bid & 3, b = bid >> 5, jb = bid & 31, kvh = jb >> 2;
  const int g = lane >> 4, c = lane & 15, hd = lane >> 5;
  asm volatile("" : "+s"(x), "+s"(Kc), "+s"(Vc), "+s"(wq), "+s"(wo), "+s"(ws), "+s"(out));
  unsigned long long pc0;
  asm volatile("s_getpc_b64 %0" : "=s"(pc0));
  STAMP(0);
  volatile LAS unsigned* ctl = (volatile LAS unsigned*)(lds + L_TAG + 8);
  if (tid < 6) ctl[tid] = 0u;
  if (__builtin_expect(wave < 4, 1)) {
    __builtin_amdgcn_s_setprio(3);
    const size_t t0 = (size_t)bid * 1048576 + wave * 8192;
    const char* src = Kc + t0;
    const unsigned ring0 = (unsigned)(size_t)lds + L_RING + wave * (RS * 8192);
    unsigned vo[8];
#pragma unroll
    for (int p = 0; p < 8; ++p) vo[p] = hd * 512 + ((((lane & 31) ^ hd) * 16) ^ (p << 5)) + (p >> 2) * 4096;
    unsigned wm = WMASK(DUTY);
#define ISW(W, p) do { if (((p) & 3) == 0) set_m0(dst + ((p) >> 2) * 4096); piece<W, (p), ((p) & 3) * 1024>(wm, vo[p], src); } while (0)
#define ISS(p) ISW(true, p)
    {
      const unsigned dst = ring0;
      ISW(false, 0); ISW(false, 1); ISW(false, 2); ISW(false, 3); ISW(false, 4); ISW(false, 5); ISW(false, 6); ISW(false, 7);
      src += 32768;
    }
    unsigned sched = now() * 10u + (unsigned)(TOFF * 10 + (RH + RS) * TAU);
    unsigned tnow = 0, adj = 0;
    bar();
#define PACE_STEP() do { tnow = now(); adj = ctl[1]; } while (0)
#define PACE_TAKE() do { const int late_ = (int)(tnow * 10u - sched); if (lane == 0) ctl[2 + wave] = (unsigned)late_; \
      int d_ = DUTY + (((late_ - (int)adj) * PG) / 1280); d_ = d_ < 0 ? 0 : d_ > 16 ? 16 : d_; wm = __builtin_amdgcn_readfirstlane(WMASK(d_)); sched += TAU; } while (0)
    const LAS char* wring = lds + L_RING + wave * (RS * 8192);
    LAS char* scw = lds + L_SC + wave * 4096 + (g * 2 + (c & 1)) * 16;
    const int kro = c * 512 + ((g ^ c) * 16);
#define KFRAG(base_, p) (*(const LAS v4u*)((base_) + ((kro ^ (((p) & 3) << 6)) + ((p) >> 2) * 256)))
    v4u hold[RH][8];
#pragma nounroll
    for (int r = 1;; ++r) {
      const int sl = r >= RS ? r - RS : r;
      if (r >= RS) {
        waitvm<8 * (RS - 1)>();
#pragma unroll
        for (int j = 0; j < RH; ++j)
          if (sl == j) {
#pragma unroll
            for (int p = 0; p < 8; ++p) hold[j][p] = KFRAG(wring + j * 8192, p);
          }
        waitlgkm();
        if (r == RS + RH - 1) break;
      }
      const unsigned dst = ring0 + sl * 8192;
      ISS(0); ISS(1); ISS(2); ISS(3); ISS(4); ISS(5); ISS(6); ISS(7);
      src += 32768;
    }
    STAMP(6);
#pragma nounroll
    for (unsigned sp = 0; sp < LSPIN_CAP && ctl[0] == 0u; ++sp) __builtin_amdgcn_s_sleep(1);
    asm volatile("" ::: "memory");
    STAMP(1);
    v4u qf[8];
    float mx = -INFINITY;
    f32x4 acc[16], accl;
    int fs = RH - 1, cs = RH % RS;
#define HOLD(r) do { if (FIRST) { f32x4 h = {0.f, 0.f, 0.f, 0.f}; _Pragma("unroll") for (int p = 0; p < 8; ++p) h = mfma32(hold[r][p], qf[p], h); \
      if (c < 2) *(LAS f32x4*)(scw + (r) * 128) = h; mx = fmaxf(fmaxf(mx, h[0]), fmaxf(fmaxf(h[1], h[2]), h[3])); PINV(mx); } } while (0)
    auto step_a = [&](auto first, int s) {
      constexpr bool FIRST = decltype(first)::value;
      waitvm<8 * (RS - 2)>();
      const LAS char* base = wring + cs * 8192;
      const unsigned dst = ring0 + fs * 8192;
      ISS(0);
      if (FIRST) {
#pragma unroll
        for (int p = 0; p < 8; ++p) qf[p] = *(const LAS v4u*)(lds + L_Q + (c & 1) * 512 + p * 64 + g * 16);
      }
      v4u kf[8];
#pragma unroll
      for (int p = 0; p < 8; ++p) kf[p] = KFRAG(base, p);
      f32x4 a = {0.f, 0.f, 0.f, 0.f};
      ISS(1);
      a = mfma32(kf[0], qf[0], a); a = mfma32(kf[1], qf[1], a); PINV(a);
      ISS(2);
      a = mfma32(kf[2], qf[2], a); a = mfma32(kf[3], qf[3], a); PINV(a);
      HOLD(0);
      ISS(3);
      a = mfma32(kf[4], qf[4], a); a = mfma32(kf[5], qf[5], a); PINV(a);
      HOLD(1);
      ISS(4);
      a = mfma32(kf[6], qf[6], a); a = mfma32(kf[7], qf[7], a); PINV(a);
      HOLD(2);
      ISS(5);
      if (c < 2) *(LAS f32x4*)(scw + s * 128) = a;
      mx = fmaxf(fmaxf(mx, a[0]), fmaxf(fmaxf(a[1], a[2]), a[3])); PINV(mx);
      HOLD(3);
      ISS(6);
      if (FIRST) {
        accl = f32x4{0.f, 0.f, 0.f, 0.f}; PINV(accl);
#pragma unroll
        for (int d = 0; d < 16; ++d) { acc[d] = f32x4{0.f, 0.f, 0.f, 0.f}; PINV(acc[d]); }
      }
      PACE_STEP();
      ISS(7);
      PACE_TAKE();
      src += 32768;
      fs = cs; cs = (cs == RS - 1) ? 0 : cs + 1;
    };
    step_a(std::true_type{}, RH);
#pragma nounroll
    for (int s = RH + 1; s < 32; ++s) {
      if (s == 33 - RS) {
        src = Vc + t0;
#pragma unroll
        for (int p = 0; p < 8; ++p) vo[p] = hd * 512 + ((((lane & 31) ^ (hd << 1)) * 16) ^ ((p & 3) << 6)) + (p >> 2) * 4096;
      }
      step_a(std::false_type{}, s);
    }
    const float M = rows_max(mx) * C2;
    STAMP(2);
    const int kq = 4 * g + ((lane >> 2) & 3);
    const int vro = kq * 512 + (kq & 7) * 32 + (lane & 3) * 8;
    const v2u ones = {0x3F803F80u, 0x3F803F80u};
#define PFRAG(sc_) v2u{cvtpk(ex2(fmaf(sc_[0], C2, -M)), ex2(fmaf(sc_[1], C2, -M))), cvtpk(ex2(fmaf(sc_[2], C2, -M)), ex2(fmaf(sc_[3], C2, -M)))}
#define PVM(p) do { acc[p] = mfma16(vf[p], pf, acc[p]); acc[(p) + 8] = mfma16(vf[(p) + 8], pf, acc[(p) + 8]); asm volatile("" : "+v"(acc[p]), "+v"(acc[(p) + 8])); } while (0)
#pragma nounroll
    for (int i = 0; i < 33 - RS; ++i) {
      waitvm<8 * (RS - 2)>();
      const LAS char* base = wring + cs * 8192;
      const unsigned dst = ring0 + fs * 8192;
      ISS(0);
      const f32x4 sc = *(const LAS f32x4*)(scw + i * 128);
      v2u vf[16];
#pragma unroll
      for (int p = 0; p < 4; ++p) { const LAS char* a = base + (vro ^ (p << 5)); vf[p] = ldtr(a); vf[p + 8] = ldtr(a + 256); }
      ISS(1);
#pragma unroll
      for (int p = 4; p < 8; ++p) { const LAS char* a = base + (vro ^ (p << 5)); vf[p] = ldtr(a); vf[p + 8] = ldtr(a + 256); }
      v2u pf = PFRAG(sc);
      PINV(pf);
      ISS(2);
      accl = mfma16(ones, pf, accl);
      PVM(0); PVM(1);
      ISS(3);
      PVM(2); PVM(3);
      ISS(4);
      PVM(4); PVM(5);
      ISS(5);
      PVM(6); PVM(7);
      ISS(6);
      PACE_STEP();
      ISS(7);
      PACE_TAKE();
      src += 32768;
      fs = cs; cs = (cs == RS - 1) ? 0 : cs + 1;
    }
#pragma nounroll
    for (int i = 33 - RS; i < 32; ++i) {
      const LAS char* base = wring + cs * 8192;
      const f32x4 sc = *(const LAS f32x4*)(scw + i * 128);
      v2u pf = PFRAG(sc);
      accl = mfma16(ones, pf, accl);
      asm volatile("" : "+v"(pf), "+v"(accl));
      if (i == 31) waitvm<0>(); else waitvm<8>();
#pragma unroll
      for (int p = 0; p < 8; ++p) {
        const LAS char* a = base + (vro ^ (p << 5));
        acc[p] = mfma16(ldtr(a), pf, acc[p]);
        acc[p + 8] = mfma16(ldtr(a + 256), pf, acc[p + 8]);
      }
      cs = (cs == RS - 1) ? 0 : cs + 1;
    }
    waitlgkm();
    if (c < 2) {
#pragma unroll
      for (int d = 0; d < 16; ++d) *(LAS f32x4*)(lds + L_RING + wave * (RS * 8192) + c * 1024 + (16 * d + 4 * g) * 4) = acc[d];
      if (g == 0) *(LAS f32x2*)(lds + L_ST + wave * 16 + c * 8) = f32x2{M, accl[0]};
    }
    STAMP(3);
    bar();
    const v2u tag = *(const LAS v2u*)(lds + L_TAG);
    {
      float Mx, Ls, o0, o1;
      merge4(lds + L_ST + hd * 8, lds + L_RING + hd * 1024 + wave * 256 + (lane & 31) * 8, RS * 8192, Mx, Ls, o0, o1);
      if (wave != m) {
        const __amdgpu_buffer_rsrc_t ro = RSRC(ws + W_INB + (size_t)(grp * 4 + wave) * 8192 + 4096 + (((m - wave) & 3) - 1) * 1152, 1152);
        __builtin_amdgcn_raw_buffer_store_b128(v4u{asu(o0), tag.x, asu(o1), tag.y}, ro, lane * 16, 0, SC1);
        if ((lane & 31) == 0) __builtin_amdgcn_raw_buffer_store_b128(v4u{asu(Mx), tag.x, asu(Ls), tag.y}, ro, 1024 + hd * 16, 0, SC1);
        STAMP(4);
        const __amdgpu_buffer_rsrc_t ri = RSRC(ws + W_INB + (size_t)bid * 8192 + 4096 + (((wave - m) & 3) - 1) * 1152, 1152);
        v4u v, s;
#pragma nounroll
        for (unsigned sp = 0; sp < SPIN_CAP; ++sp) {
          v = poll128(ri, lane * 16);
          s = poll128(ri, 1024 + hd * 16);
          if (__all((v.y == tag.x) & (v.w == tag.y) & (s.y == tag.x) & (s.w == tag.y))) break;
        }
        o0 = asf(v.x); o1 = asf(v.z); Mx = asf(s.x); Ls = asf(s.z);
      } else {
        const v2u u = poll64(RSRC(ws + W_INB + (size_t)bid * 8192, 4096), lane * 64);
        asm volatile("" ::"v"(u));
      }
      *(LAS f32x2*)(lds + L_RX + wave * 512 + lane * 8) = f32x2{o0, o1};
      if ((lane & 31) == 0) *(LAS f32x2*)(lds + L_RS + wave * 16 + hd * 8) = f32x2{Mx, Ls};
    }
    bar();
    if (wave == 0 && jb < 28) {
      const __amdgpu_buffer_rsrc_t rs = RSRC(ws + W_INB + (size_t)bid * 8192, 4096);
      v4u v[4];
      __builtin_amdgcn_s_sleep(12);
#pragma nounroll
      for (unsigned sp = 0; sp < SPIN_CAP; ++sp) {
#pragma unroll
        for (int it = 0; it < 4; ++it) v[it] = poll128(rs, it * 1024 + lane * 16);
        bool ok = true;
#pragma unroll
        for (int it = 0; it < 4; ++it) ok &= (v[it].y == tag.x) & (v[it].w == tag.y);
        if (__all(ok)) break;
      }
      float s0 = (asf(v[0].x) + asf(v[1].x)) + (asf(v[2].x) + asf(v[3].x));
      float s1 = (asf(v[0].z) + asf(v[1].z)) + (asf(v[2].z) + asf(v[3].z));
      s0 += asf(__builtin_amdgcn_mov_dpp((int)asu(s0), 0x128, 0xf, 0xf, true));
      s1 += asf(__builtin_amdgcn_mov_dpp((int)asu(s1), 0x128, 0xf, 0xf, true));
      s0 = rows_sum(s0); s1 = rows_sum(s1);
      if (lane < 8) ((GAS unsigned*)out)[b * (DM / 2) + jb * 8 + lane] = cvtpk(s0, s1);
      STAMP(5);
    }
  } else {
    const int hw = wave - 4;
    bar();
    const unsigned tk0 = now();
    if (hw == 0) {
      const int cb = (bid & 7) * 32 + (bid >> 3);
      const __amdgpu_buffer_rsrc_t rq = RSRC(ws + W_QG, 131072);
      const int qin = b * 16384 + kvh * 2048 + lane * 16, qout = (c & 7) * 16384 + (cb * 4 + g) * 16;
      v4u o0 = poll128(rq, qin), o1 = poll128(rq, qin + 1024), mine = poll128(rq, qout);
      const unsigned l0 = (unsigned)(size_t)lds;
      const unsigned voff = (lane >> 1) * 8192 + (lane & 1) * 16;
      const LAS char* lp = lds + L_SC + (8 * g + ((lane >> 2) & 3)) * 32 + (lane & 3) * 8;
      const LAS char* lx = lds + L_X + (c & 7) * (DM * 2) + g * 16;
      unsigned long long miss0 = 0, miss1 = 0;
      int blk = cb;
#pragma nounroll
      for (int pass = 0;; ++pass) {
        const char* wsrc = wq + blk * 32;
#pragma nounroll
        for (int s = 0; s < 14; ++s) { set_m0(l0 + L_SC + s * 1024); dma1(voff, wsrc); wsrc += 32 * 8192; }
        if (pass == 0) {
#pragma nounroll
          for (int s = 0; s < 7; ++s) { set_m0(l0 + L_X + s * 1024); dma1(lane * 16, x + s * 1024); }
        }
        asm volatile("s_waitcnt vmcnt(0)" : "+v"(o0), "+v"(o1), "+v"(mine)::"memory");
        f32x4 qa = {0.f, 0.f, 0.f, 0.f}, qb = {0.f, 0.f, 0.f, 0.f};
#pragma nounroll
        for (int s = 0; s < 14; s += 2) {
          const v2u a0 = ldtr(lp + s * 1024), a1 = ldtr(lp + s * 1024 + 128), b0 = ldtr(lp + s * 1024 + 1024), b1 = ldtr(lp + s * 1024 + 1152);
          qa = mfma32(v4u{a0.x, a0.y, a1.x, a1.y}, *(const LAS v4u*)(lx + s * 64), qa);
          qb = mfma32(v4u{b0.x, b0.y, b1.x, b1.y}, *(const LAS v4u*)(lx + s * 64 + 64), qb);
        }
        qa += qb;
        const v2u qq = {cvtpk(qa[0], qa[1]), cvtpk(qa[2], qa[3])};
        if (pass == 0) {
          STAMP(6);
          if (c < 8) __builtin_amdgcn_raw_buffer_store_b128(v4u{qq.x, mine.y + 1u, qq.y, mine.w + 1u}, rq, qout, 0, SC1);
          v4u v0, v1;
#pragma nounroll
          for (unsigned sp = 0;; ++sp) {
            __builtin_amdgcn_s_sleep(8);
            v0 = poll128(rq, qin);
            v1 = poll128(rq, qin + 1024);
            if (sp >= QSPIN || __all((v0.y != o0.y) & (v0.w != o0.w) & (v1.y != o1.y) & (v1.w != o1.w))) break;
          }
          *(LAS v2u*)(lds + L_Q + lane * 8) = v2u{v0.x, v0.z};
          *(LAS v2u*)(lds + L_Q + 512 + lane * 8) = v2u{v1.x, v1.z};
          miss0 = __ballot((v0.y == o0.y) | (v0.w == o0.w));
          miss1 = __ballot((v1.y == o1.y) | (v1.w == o1.w));
        } else {
          if (c == b) *(LAS v2u*)(lds + L_Q + (blk & 31) * 32 + g * 8) = qq;
        }
        if (miss0) { const int j = __builtin_ctzll(miss0) >> 2; miss0 &= ~(15ull << (4 * j)); blk = kvh * 32 + j; }
        else if (miss1) { const int j = __builtin_ctzll(miss1) >> 2; miss1 &= ~(15ull << (4 * j)); blk = kvh * 32 + 16 + j; }
        else break;
        waitlgkm();
      }
      waitlgkm();
      if (lane == 0) ctl[0] = 1u;
      STAMP(2);
    } else {
#pragma nounroll
      for (unsigned sp = 0; sp < LSPIN_CAP && ctl[0] == 0u; ++sp) __builtin_amdgcn_s_sleep(16);
    }
    if (hw == 1) {
      const unsigned long long clk = __builtin_amdgcn_s_memrealtime();
      const unsigned n0 = (unsigned)clk, n1 = (unsigned)(clk >> 8) & ~1u;
      const __amdgpu_buffer_rsrc_t rr = RSRC(ws + W_REQ + (size_t)bid * 256, 256);
      if (lane == 0) __builtin_amdgcn_raw_buffer_store_b64(v2u{n0, n1}, rr, 0, 0, SC1);
      v4u rep = {0u, 0u, 0u, 0u};
#pragma nounroll
      for (unsigned sp = 0; sp < SPIN_CAP; ++sp) {
        __builtin_amdgcn_s_sleep(64);
        rep = poll128(rr, 128);
        if (__builtin_amdgcn_readfirstlane(rep.y) == n0 && __builtin_amdgcn_readfirstlane(rep.w) == n1) break;
      }
      if (lane == 0) {
        __builtin_amdgcn_raw_buffer_store_b64(v2u{rep.x, rep.z}, rr, 0, 0, SC1);
        *(LAS v2u*)(lds + L_TAG) = v2u{rep.x, rep.z};
      }
      STAMP(2);
    } else if (hw == 2 && jb == 0) {
      const unsigned long long clk = __builtin_amdgcn_s_memrealtime();
      const unsigned t0 = (unsigned)clk, t1 = (unsigned)(clk >> 8) | 1u;
      const __amdgpu_buffer_rsrc_t rl = RSRC(ws + W_REQ + (size_t)bid * 256, 32 * 256);
      v2u last = {t0, t1};
#pragma nounroll
      for (unsigned sp = 0; sp < SPIN_CAP; ++sp) {
        __builtin_amdgcn_s_sleep(32);
        const v2u r = poll64(rl, (lane & 31) * 256);
        const bool d = (r.x == t0) & (r.y == t1);
        const bool plaus = (sp >= 64) | (r.x - t0 + 0x100000u < 0x200000u);
        if (lane < 32 && !d && plaus && (r.x != last.x || r.y != last.y)) {
          __builtin_amdgcn_raw_buffer_store_b128(v4u{t0, r.x, t1, r.y}, rl, lane * 256 + 128, 0, SC1);
          last = r;
        }
        if (__all(d)) break;
      }
    }
    const int ch = c < 14 ? c : 13;
    const char* wp = wo + (size_t)((2 * kvh + (g >> 1)) * 256 + 64 * m + 32 * (g & 1)) * (DM * 2) + hw * 224 + ch * 16;
    unsigned wr[16][8];
#pragma unroll
    for (int i0 = 0; i0 < 16; i0 += 2) {
      v4u r[4];
#pragma unroll
      for (int u = 0; u < 4; ++u) r[u] = *(const GAS v4u*)(wp + (size_t)(2 * i0 + u) * (DM * 2));
#pragma unroll
      for (int u = 0; u < 2; ++u) {
#pragma unroll
        for (int e = 0; e < 4; ++e) {
          wr[i0 + u][2 * e] = __builtin_amdgcn_perm(r[2 * u + 1][e], r[2 * u][e], 0x05040100u);
          wr[i0 + u][2 * e + 1] = __builtin_amdgcn_perm(r[2 * u + 1][e], r[2 * u][e], 0x07060302u);
        }
        asm volatile("" : "+v"(wr[i0 + u][0]), "+v"(wr[i0 + u][1]), "+v"(wr[i0 + u][2]), "+v"(wr[i0 + u][3]), "+v"(wr[i0 + u][4]), "+v"(wr[i0 + u][5]), "+v"(wr[i0 + u][6]), "+v"(wr[i0 + u][7])::"memory");
      }
    }
    if (hw == 3) {
      const __amdgpu_buffer_rsrc_t rc = RSRC(ws + W_CK, 64 * 4352);
#pragma nounroll
      for (int k = 0; k < CROUNDS; ++k) {
        const unsigned tgo = tk0 + CT0 + k * CSTEP;
        idle_until(tgo);
        if (bid % 17 == k && lane < 4) {
          const int l4 = ((int)ctl[2] + (int)ctl[3] + (int)ctl[4] + (int)ctl[5]) >> 2;
          __builtin_amdgcn_raw_buffer_store_b64(v2u{(unsigned)l4, tgo}, rc, (k * 4 + lane) * 4352 + (bid / 17) * 8, 0, SC1);
        }
        idle_until(tgo + CLAG);
        const v2u r = poll64(rc, (k * 4 + ((bid >> 3) & 3)) * 4352 + c * 8);
        const bool ok = (r.y - tgo + 500u < 1000u) & ((int)r.x > -20000) & ((int)r.x < 20000);
        float sum = ok ? (float)(int)r.x : 0.f, cnt = ok ? 1.f : 0.f;
#pragma unroll
        for (int o = 1; o < 16; o <<= 1) { sum += __shfl_xor(sum, o); cnt += __shfl_xor(cnt, o); }
        if (cnt >= 8.f && lane == 0) ctl[1] = (unsigned)(int)(sum / cnt);
      }
    }
    if (hw == 2) {
      idle_until(tk0 + 6400);
      const GAS char* cp = (const GAS char*)(pc0 & ~127ull) + lane * 128;
      const unsigned t0 = *(const volatile GAS unsigned*)cp, t1 = *(const volatile GAS unsigned*)(cp + CODE_HI);
      asm volatile("" ::"v"(t0), "v"(t1));
    }
    STAMP(1);
    bar();
    const v2u tag = *(const LAS v2u*)(lds + L_TAG);
    STAMP(3);
    bar();
    STAMP(4);
    float Mx, Ls, o0, o1;
    merge4(lds + L_RS + hd * 8, lds + L_RX + lane * 8, 512, Mx, Ls, o0, o1);
    const float inv = __builtin_amdgcn_rcpf(Ls);
    const unsigned op = cvtpk(o0 * inv, o1 * inv);
    float y[8];
#pragma unroll
    for (int e = 0; e < 8; ++e) y[e] = 0.f;
#define STEP(i) { const unsigned ob = row_bcast<i>(op); _Pragma("unroll") for (int e = 0; e < 8; ++e) y[e] = dot2(wr[i][e], ob, y[e]); }
    STEP(0) STEP(1) STEP(2) STEP(3) STEP(4) STEP(5) STEP(6) STEP(7) STEP(8) STEP(9) STEP(10) STEP(11) STEP(12) STEP(13) STEP(14) STEP(15)
#undef STEP
    float z[4];
#pragma unroll
    for (int e = 0; e < 4; ++e) { auto s = __builtin_amdgcn_permlane32_swap(asu(y[e]), asu(y[e + 4]), false, false); z[e] = asf(s[0]) + asf(s[1]); }
    float u[2];
#pragma unroll
    for (int e = 0; e < 2; ++e) { auto s = __builtin_amdgcn_permlane16_swap(asu(z[e]), asu(z[e + 2]), false, false); u[e] = asf(s[0]) + asf(s[1]); }
    if (c < 14) {
      const int n0 = hw * 112 + c * 8 + g * 2;
      const __amdgpu_buffer_rsrc_t ri = RSRC(ws + W_INB + (size_t)b * (32 * 8192), 32 * 8192);
      __builtin_amdgcn_raw_buffer_store_b128(v4u{asu(u[0]), tag.x, asu(u[1]), tag.y}, ri, (n0 >> 4) * 8192 + jb * 128 + (n0 & 15) * 8, 0, SC1);
    }
    STAMP(5);
  }
  STAMP_FLUSH();
}

extern "C" void kernel_launch(void* const* d_in, const int* in_sizes, int n_in, void* d_out, int out_size, void* d_ws, size_t ws_size, hipStream_t stream) {
  (void)hipFuncSetAttribute((const void*)k_main, hipFuncAttributeMaxDynamicSharedMemorySize, L_END);
  k_main<<<256, 512, L_END, stream>>>((const char*)d_in[0], (const char*)d_in[1], (const char*)d_in[2], (const char*)d_in[3], (const char*)d_in[4], (char*)d_ws, (unsigned*)d_out);
}
```

```cpp
#include <hip/hip_runtime.h>
#include <hip/hip_bf16.h>
#include <type_traits>

typedef unsigned v4u __attribute__((ext_vector_type(4)));
typedef unsigned v2u __attribute__((ext_vector_type(2)));
typedef float f32x4 __attribute__((ext_vector_type(4)));
typedef float f32x2 __attribute__((ext_vector_type(2)));
typedef short bf16x8 __attribute__((ext_vector_type(8)));
typedef short s16x4 __attribute__((ext_vector_type(4)));
typedef __bf16 bf16x2 __attribute__((ext_vector_type(2)));
#define LAS __attribute__((address_space(3)))
#define GAS __attribute__((address_space(1)))

constexpr int DM = 448;
constexpr float C2 = 0.0625f * 1.4426950408889634f;
constexpr int RS = 4;
constexpr int RH = 4;
#define WLO 4
#define WMID 6
#define WHI 8
#define DUTY 8
#define TAU 1080
#define TOFF 0
#define PG 12
#define QSPIN 16
#define CROUNDS 4
#define CT0 2000
#define CSTEP 1200
#define CLAG 250
#define CODE_HI 4480
constexpr unsigned SPIN_CAP = 1u << 18, LSPIN_CAP = 1u << 24;

constexpr size_t W_REQ = 0;
constexpr size_t W_QG = 65536;
constexpr size_t W_INB = W_QG + 131072;
constexpr size_t W_CK = W_INB + 256 * 8192;

constexpr int L_RING = 0;
constexpr int L_SC = 4 * RS * 8192;
constexpr int L_Q = L_SC + 16384;
constexpr int L_ST = L_Q + 1024;
constexpr int L_RX = L_ST + 64;
constexpr int L_RS = L_RX + 2048;
constexpr int L_TAG = L_RS + 64;
constexpr int L_X = L_TAG + 64;
constexpr int L_END = L_X + 7168;

template <int N> __device__ __forceinline__ void waitvm() { asm volatile("s_waitcnt vmcnt(%0)" ::"i"(N) : "memory"); }
__device__ __forceinline__ void waitlgkm() { asm volatile("s_waitcnt lgkmcnt(0)" ::: "memory"); }
__device__ __forceinline__ void bar() { asm volatile("s_waitcnt lgkmcnt(0)\n\ts_barrier" ::: "memory"); }
__device__ __forceinline__ unsigned now() { return (unsigned)__builtin_amdgcn_s_memrealtime(); }
__device__ __forceinline__ void set_m0(unsigned v) { asm volatile("s_mov_b32 m0, %0\n\ts_nop 0" ::"s"(v) : "memory"); }
__device__ __forceinline__ void dma1(unsigned voff, const void* sbase) { asm volatile("global_load_lds_dwordx4 %0, %1" ::"v"(voff), "s"(sbase) : "memory"); }
#define BR3(p) ((((p) & 1) << 2) | ((p) & 2) | (((p) >> 2) & 1))
#define WMASK(d) ((1u << (d)) - 1u)
template <bool WAIT, int P, int OFF> __device__ __forceinline__ void piece(unsigned wm, unsigned voff, const void* sbase) {
  if constexpr (WAIT)
    asm volatile(
        "s_waitcnt vmcnt(%5)\n\t"
        "s_bitcmp1_b32 %0, %7\n\t"
        "s_cbranch_scc1 1f\n\t"
        "s_waitcnt vmcnt(%4)\n\t"
        "s_bitcmp1_b32 %0, %6\n\t"
        "s_cbranch_scc1 1f\n\t"
        "s_waitcnt vmcnt(%3)\n"
        "1:\n\t"
        "global_load_lds_dwordx4 %1, %2 offset:%8 nt" ::"s"(wm), "v"(voff), "s"(sbase), "i"(WLO - 1), "i"(WMID - 1), "i"(WHI - 1), "i"(BR3(P)), "i"(BR3(P) + 8), "i"(OFF)
        : "memory", "scc");
  else
    asm volatile("global_load_lds_dwordx4 %0, %1 offset:%2 nt" ::"v"(voff), "s"(sbase), "i"(OFF) : "memory");
}
__device__ __forceinline__ f32x4 mfma32(v4u a, v4u b, f32x4 c) {
  return __builtin_amdgcn_mfma_f32_16x16x32_bf16(__builtin_bit_cast(bf16x8, a), __builtin_bit_cast(bf16x8, b), c, 0, 0, 0);
}
__device__ __forceinline__ f32x4 mfma16(v2u a, v2u b, f32x4 c) {
  return __builtin_amdgcn_mfma_f32_16x16x16bf16_1k(__builtin_bit_cast(s16x4, a), __builtin_bit_cast(s16x4, b), c, 0, 0, 0);
}
__device__ __forceinline__ v2u ldtr(const LAS char* p) { return __builtin_bit_cast(v2u, __builtin_amdgcn_ds_read_tr16_b64_v4i16((LAS s16x4*)p)); }
__device__ __forceinline__ unsigned cvtpk(float lo, float hi) { return __builtin_bit_cast(unsigned, __builtin_convertvector(f32x2{lo, hi}, bf16x2)); }
__device__ __forceinline__ float dot2(unsigned a, unsigned b, float acc) {
  return __builtin_amdgcn_fdot2_f32_bf16(__builtin_bit_cast(bf16x2, a), __builtin_bit_cast(bf16x2, b), acc, false);
}
__device__ __forceinline__ float asf(unsigned u) { return __builtin_bit_cast(float, u); }
__device__ __forceinline__ unsigned asu(float f) { return __builtin_bit_cast(unsigned, f); }
__device__ __forceinline__ float ex2(float x) { return __builtin_amdgcn_exp2f(x); }
__device__ __forceinline__ float rows_max(float x) {
  auto s = __builtin_amdgcn_permlane16_swap(asu(x), asu(x), false, false);
  x = fmaxf(asf(s[0]), asf(s[1]));
  auto t = __builtin_amdgcn_permlane32_swap(asu(x), asu(x), false, false);
  return fmaxf(asf(t[0]), asf(t[1]));
}
__device__ __forceinline__ float rows_sum(float x) {
  auto s = __builtin_amdgcn_permlane16_swap(asu(x), asu(x), false, false);
  x = asf(s[0]) + asf(s[1]);
  auto t = __builtin_amdgcn_permlane32_swap(asu(x), asu(x), false, false);
  return asf(t[0]) + asf(t[1]);
}
template <int N> __device__ __forceinline__ unsigned row_bcast(unsigned x) { return (unsigned)__builtin_amdgcn_mov_dpp((int)x, 0x150 + N, 0xf, 0xf, true); }
#define RSRC(p, n) __builtin_amdgcn_make_buffer_rsrc((void*)(p), 0, (int)(n), 0x00020000)
constexpr int SC1 = 16;
__device__ __forceinline__ v4u poll128(__amdgpu_buffer_rsrc_t rs, int off) {
  asm volatile("" : "+v"(off)::"memory");
  return __builtin_amdgcn_raw_buffer_load_b128(rs, off, 0, SC1);
}
__device__ __forceinline__ v2u poll64(__amdgpu_buffer_rsrc_t rs, int off) {
  asm volatile("" : "+v"(off)::"memory");
  return __builtin_amdgcn_raw_buffer_load_b64(rs, off, 0, SC1);
}
__device__ __forceinline__ void idle_until(unsigned t) {
#pragma nounroll
  for (unsigned sp = 0; sp < 4096u && (int)(now() - t) < 0; ++sp) __builtin_amdgcn_s_sleep(16);
}
#define PINV(x) asm volatile("" : "+v"(x))
#define STAMP(i) do {} while (0)
#define STAMP_FLUSH() do {} while (0)

__device__ __forceinline__ void merge4(const LAS char* stp, const LAS char* pap, int pstride, float& Mx, float& Ls, float& o0, float& o1) {
  f32x2 st[4], pa[4];
#pragma unroll
  for (int w = 0; w < 4; ++w) {
    st[w] = *(const LAS f32x2*)(stp + w * 16);
    pa[w] = *(const LAS f32x2*)(pap + w * pstride);
  }
  Mx = fmaxf(fmaxf(st[0].x, st[1].x), fmaxf(st[2].x, st[3].x));
  Ls = 0.f; o0 = 0.f; o1 = 0.f;
#pragma unroll
  for (int w = 0; w < 4; ++w) {
    const float f = ex2(st[w].x - Mx);
    Ls = fmaf(f, st[w].y, Ls); o0 = fmaf(f, pa[w].x, o0); o1 = fmaf(f, pa[w].y, o1);
  }
}

__global__ void __launch_bounds__(512, 2) k_main(const char* x, const char* Kc, const char* Vc, const char* wq, const char* wo, char* ws, unsigned* out) {
  extern __shared__ __attribute__((aligned(16))) char lds_[];
  LAS char* lds = (LAS char*)lds_;
  const int tid = threadIdx.x, lane = tid & 63, wave = __builtin_amdgcn_readfirstlane(tid >> 6);
  const int bid = blockIdx.x, grp = bid >> 2, m = bid & 3, b = bid >> 5, jb = bid & 31, kvh = jb >> 2;
  const int g = lane >> 4, c = lane & 15, hd = lane >> 5;
  asm volatile("" : "+s"(x), "+s"(Kc), "+s"(Vc), "+s"(wq), "+s"(wo), "+s"(ws), "+s"(out));
  unsigned long long pc0;
  asm volatile("s_getpc_b64 %0" : "=s"(pc0));
  STAMP(0);
  volatile LAS unsigned* ctl = (volatile LAS unsigned*)(lds + L_TAG + 8);
  if (tid < 6) ctl[tid] = 0u;
  if (__builtin_expect(wave < 4, 1)) {
    __builtin_amdgcn_s_setprio(3);
    const size_t t0 = (size_t)(bid >> 1) * 2097152 + (bid & 1) * 32768 + wave * 8192;
    const char* src = Kc + t0;
    const unsigned ring0 = (unsigned)(size_t)lds + L_RING + wave * (RS * 8192);
    unsigned vo[8];
#pragma unroll
    for (int p = 0; p < 8; ++p) vo[p] = hd * 512 + ((((lane & 31) ^ hd) * 16) ^ (p << 5)) + (p >> 2) * 4096;
    unsigned wm = WMASK(DUTY);
#define ISW(W, p) do { if (((p) & 3) == 0) set_m0(dst + ((p) >> 2) * 4096); piece<W, (p), ((p) & 3) * 1024>(wm, vo[p], src); } while (0)
#define ISS(p) ISW(true, p)
    {
      const unsigned dst = ring0;
      ISW(false, 0); ISW(false, 1); ISW(false, 2); ISW(false, 3); ISW(false, 4); ISW(false, 5); ISW(false, 6); ISW(false, 7);
      src += 65536;
    }
    unsigned sched = now() * 10u + (unsigned)(TOFF * 10 + (RH + RS) * TAU);
    unsigned tnow = 0, adj = 0;
    bar();
#define PACE_STEP() do { tnow = now(); adj = ctl[1]; } while (0)
#define PACE_TAKE() do { const int late_ = (int)(tnow * 10u - sched); if (lane == 0) ctl[2 + wave] = (unsigned)late_; \
      int d_ = DUTY + (((late_ - (int)adj) * PG) / 1280); d_ = d_ < 0 ? 0 : d_ > 16 ? 16 : d_; wm = __builtin_amdgcn_readfirstlane(WMASK(d_)); sched += TAU; } while (0)
    const LAS char* wring = lds + L_RING + wave * (RS * 8192);
    LAS char* scw = lds + L_SC + wave * 4096 + (g * 2 + (c & 1)) * 16;
    const int kro = c * 512 + ((g ^ c) * 16);
#define KFRAG(base_, p) (*(const LAS v4u*)((base_) + ((kro ^ (((p) & 3) << 6)) + ((p) >> 2) * 256)))
    v4u hold[RH][8];
#pragma nounroll
    for (int r = 1;; ++r) {
      const int sl = r >= RS ? r - RS : r;
      if (r >= RS) {
        waitvm<8 * (RS - 1)>();
#pragma unroll
        for (int j = 0; j < RH; ++j)
          if (sl == j) {
#pragma unroll
            for (int p = 0; p < 8; ++p) hold[j][p] = KFRAG(wring + j * 8192, p);
          }
        waitlgkm();
        if (r == RS + RH - 1) break;
      }
      const unsigned dst = ring0 + sl * 8192;
      ISS(0); ISS(1); ISS(2); ISS(3); ISS(4); ISS(5); ISS(6); ISS(7);
      src += 65536;
    }
    STAMP(6);
#pragma nounroll
    for (unsigned sp = 0; sp < LSPIN_CAP && ctl[0] == 0u; ++sp) __builtin_amdgcn_s_sleep(1);
    asm volatile("" ::: "memory");
    STAMP(1);
    v4u qf[8];
    float mx = -INFINITY;
    f32x4 acc[16], accl;
    int fs = RH - 1, cs = RH % RS;
#define HOLD(r) do { if (FIRST) { f32x4 h = {0.f, 0.f, 0.f, 0.f}; _Pragma("unroll") for (int p = 0; p < 8; ++p) h = mfma32(hold[r][p], qf[p], h); \
      if (c < 2) *(LAS f32x4*)(scw + (r) * 128) = h; mx = fmaxf(fmaxf(mx, h[0]), fmaxf(fmaxf(h[1], h[2]), h[3])); PINV(mx); } } while (0)
    auto step_a = [&](auto first, int s) {
      constexpr bool FIRST = decltype(first)::value;
      waitvm<8 * (RS - 2)>();
      const LAS char* base = wring + cs * 8192;
      const unsigned dst = ring0 + fs * 8192;
      ISS(0);
      if (FIRST) {
#pragma unroll
        for (int p = 0; p < 8; ++p) qf[p] = *(const LAS v4u*)(lds + L_Q + (c & 1) * 512 + p * 64 + g * 16);
      }
      v4u kf[8];
#pragma unroll
      for (int p = 0; p < 8; ++p) kf[p] = KFRAG(base, p);
      f32x4 a = {0.f, 0.f, 0.f, 0.f};
      ISS(1);
      a = mfma32(kf[0], qf[0], a); a = mfma32(kf[1], qf[1], a); PINV(a);
      ISS(2);
      a = mfma32(kf[2], qf[2], a); a = mfma32(kf[3], qf[3], a); PINV(a);
      HOLD(0);
      ISS(3);
      a = mfma32(kf[4], qf[4], a); a = mfma32(kf[5], qf[5], a); PINV(a);
      HOLD(1);
      ISS(4);
      a = mfma32(kf[6], qf[6], a); a = mfma32(kf[7], qf[7], a); PINV(a);
      HOLD(2);
      ISS(5);
      if (c < 2) *(LAS f32x4*)(scw + s * 128) = a;
      mx = fmaxf(fmaxf(mx, a[0]), fmaxf(fmaxf(a[1], a[2]), a[3])); PINV(mx);
      HOLD(3);
      ISS(6);
      if (FIRST) {
        accl = f32x4{0.f, 0.f, 0.f, 0.f}; PINV(accl);
#pragma unroll
        for (int d = 0; d < 16; ++d) { acc[d] = f32x4{0.f, 0.f, 0.f, 0.f}; PINV(acc[d]); }
      }
      PACE_STEP();
      ISS(7);
      PACE_TAKE();
      src += 65536;
      fs = cs; cs = (cs == RS - 1) ? 0 : cs + 1;
    };
    step_a(std::true_type{}, RH);
#pragma nounroll
    for (int s = RH + 1; s < 32; ++s) {
      if (s == 33 - RS) {
        src = Vc + t0;
#pragma unroll
        for (int p = 0; p < 8; ++p) vo[p] = hd * 512 + ((((lane & 31) ^ (hd << 1)) * 16) ^ ((p & 3) << 6)) + (p >> 2) * 4096;
      }
      step_a(std::false_type{}, s);
    }
    const float M = rows_max(mx) * C2;
    STAMP(2);
    const int kq = 4 * g + ((lane >> 2) & 3);
    const int vro = kq * 512 + (kq & 7) * 32 + (lane & 3) * 8;
    const v2u ones = {0x3F803F80u, 0x3F803F80u};
#define PFRAG(sc_) v2u{cvtpk(ex2(fmaf(sc_[0], C2, -M)), ex2(fmaf(sc_[1], C2, -M))), cvtpk(ex2(fmaf(sc_[2], C2, -M)), ex2(fmaf(sc_[3], C2, -M)))}
#define PVM(p) do { acc[p] = mfma16(vf[p], pf, acc[p]); acc[(p) + 8] = mfma16(vf[(p) + 8], pf, acc[(p) + 8]); asm volatile("" : "+v"(acc[p]), "+v"(acc[(p) + 8])); } while (0)
#pragma nounroll
    for (int i = 0; i < 33 - RS; ++i) {
      waitvm<8 * (RS - 2)>();
      const LAS char* base = wring + cs * 8192;
      const unsigned dst = ring0 + fs * 8192;
      ISS(0);
      const f32x4 sc = *(const LAS f32x4*)(scw + i * 128);
      v2u vf[16];
#pragma unroll
      for (int p = 0; p < 4; ++p) { const LAS char* a = base + (vro ^ (p << 5)); vf[p] = ldtr(a); vf[p + 8] = ldtr(a + 256); }
      ISS(1);
#pragma unroll
      for (int p = 4; p < 8; ++p) { const LAS char* a = base + (vro ^ (p << 5)); vf[p] = ldtr(a); vf[p + 8] = ldtr(a + 256); }
      v2u pf = PFRAG(sc);
      PINV(pf);
      ISS(2);
      accl = mfma16(ones, pf, accl);
      PVM(0); PVM(1);
      ISS(3);
      PVM(2); PVM(3);
      ISS(4);
      PVM(4); PVM(5);
      ISS(5);
      PVM(6); PVM(7);
      ISS(6);
      PACE_STEP();
      ISS(7);
      PACE_TAKE();
      src += 65536;
      fs = cs; cs = (cs == RS - 1) ? 0 : cs + 1;
    }
#pragma nounroll
    for (int i = 33 - RS; i < 32; ++i) {
      const LAS char* base = wring + cs * 8192;
      const f32x4 sc = *(const LAS f32x4*)(scw + i * 128);
      v2u pf = PFRAG(sc);
      accl = mfma16(ones, pf, accl);
      asm volatile("" : "+v"(pf), "+v"(accl));
      if (i == 31) waitvm<0>(); else waitvm<8>();
#pragma unroll
      for (int p = 0; p < 8; ++p) {
        const LAS char* a = base + (vro ^ (p << 5));
        acc[p] = mfma16(ldtr(a), pf, acc[p]);
        acc[p + 8] = mfma16(ldtr(a + 256), pf, acc[p + 8]);
      }
      cs = (cs == RS - 1) ? 0 : cs + 1;
    }
    waitlgkm();
    if (c < 2) {
#pragma unroll
      for (int d = 0; d < 16; ++d) *(LAS f32x4*)(lds + L_RING + wave * (RS * 8192) + c * 1024 + (16 * d + 4 * g) * 4) = acc[d];
      if (g == 0) *(LAS f32x2*)(lds + L_ST + wave * 16 + c * 8) = f32x2{M, accl[0]};
    }
    STAMP(3);
    bar();
    const v2u tag = *(const LAS v2u*)(lds + L_TAG);
    {
      float Mx, Ls, o0, o1;
      merge4(lds + L_ST + hd * 8, lds + L_RING + hd * 1024 + wave * 256 + (lane & 31) * 8, RS * 8192, Mx, Ls, o0, o1);
      if (wave != m) {
        const __amdgpu_buffer_rsrc_t ro = RSRC(ws + W_INB + (size_t)(grp * 4 + wave) * 8192 + 4096 + (((m - wave) & 3) - 1) * 1152, 1152);
        __builtin_amdgcn_raw_buffer_store_b128(v4u{asu(o0), tag.x, asu(o1), tag.y}, ro, lane * 16, 0, SC1);
        if ((lane & 31) == 0) __builtin_amdgcn_raw_buffer_store_b128(v4u{asu(Mx), tag.x, asu(Ls), tag.y}, ro, 1024 + hd * 16, 0, SC1);
        STAMP(4);
        const __amdgpu_buffer_rsrc_t ri = RSRC(ws + W_INB + (size_t)bid * 8192 + 4096 + (((wave - m) & 3) - 1) * 1152, 1152);
        v4u v, s;
#pragma nounroll
        for (unsigned sp = 0; sp < SPIN_CAP; ++sp) {
          v = poll128(ri, lane * 16);
          s = poll128(ri, 1024 + hd * 16);
          if (__all((v.y == tag.x) & (v.w == tag.y) & (s.y == tag.x) & (s.w == tag.y))) break;
        }
        o0 = asf(v.x); o1 = asf(v.z); Mx = asf(s.x); Ls = asf(s.z);
      } else {
        const v2u u = poll64(RSRC(ws + W_INB + (size_t)bid * 8192, 4096), lane * 64);
        asm volatile("" ::"v"(u));
      }
      *(LAS f32x2*)(lds + L_RX + wave * 512 + lane * 8) = f32x2{o0, o1};
      if ((lane & 31) == 0) *(LAS f32x2*)(lds + L_RS + wave * 16 + hd * 8) = f32x2{Mx, Ls};
    }
    bar();
    if (wave == 0 && jb < 28) {
      const __amdgpu_buffer_rsrc_t rs = RSRC(ws + W_INB + (size_t)bid * 8192, 4096);
      v4u v[4];
      __builtin_amdgcn_s_sleep(12);
#pragma nounroll
      for (unsigned sp = 0; sp < SPIN_CAP; ++sp) {
#pragma unroll
        for (int it = 0; it < 4; ++it) v[it] = poll128(rs, it * 1024 + lane * 16);
        bool ok = true;
#pragma unroll
        for (int it = 0; it < 4; ++it) ok &= (v[it].y == tag.x) & (v[it].w == tag.y);
        if (__all(ok)) break;
      }
      float s0 = (asf(v[0].x) + asf(v[1].x)) + (asf(v[2].x) + asf(v[3].x));
      float s1 = (asf(v[0].z) + asf(v[1].z)) + (asf(v[2].z) + asf(v[3].z));
      s0 += asf(__builtin_amdgcn_mov_dpp((int)asu(s0), 0x128, 0xf, 0xf, true));
      s1 += asf(__builtin_amdgcn_mov_dpp((int)asu(s1), 0x128, 0xf, 0xf, true));
      s0 = rows_sum(s0); s1 = rows_sum(s1);
      if (lane < 8) __builtin_amdgcn_raw_buffer_store_b32(cvtpk(s0, s1), RSRC(out, 8 * DM * 2), (b * (DM / 2) + jb * 8 + lane) * 4, 0, 17);
      STAMP(5);
    }
  } else {
    const int hw = wave - 4;
    bar();
    const unsigned tk0 = now();
    if (hw == 0) {
      const int cb = (bid & 7) * 32 + (bid >> 3);
      const __amdgpu_buffer_rsrc_t rq = RSRC(ws + W_QG, 131072);
      const int qin = b * 16384 + kvh * 2048 + lane * 16, qout = (c & 7) * 16384 + (cb * 4 + g) * 16;
      v4u o0 = poll128(rq, qin), o1 = poll128(rq, qin + 1024), mine = poll128(rq, qout);
      const unsigned l0 = (unsigned)(size_t)lds;
      const unsigned voff = (lane >> 1) * 8192 + (lane & 1) * 16;
      const LAS char* lp = lds + L_SC + (8 * g + ((lane >> 2) & 3)) * 32 + (lane & 3) * 8;
      const LAS char* lx = lds + L_X + (c & 7) * (DM * 2) + g * 16;
      unsigned long long miss0 = 0, miss1 = 0;
      int blk = cb;
#pragma nounroll
      for (int pass = 0;; ++pass) {
        const char* wsrc = wq + blk * 32;
#pragma nounroll
        for (int s = 0; s < 14; ++s) { set_m0(l0 + L_SC + s * 1024); dma1(voff, wsrc); wsrc += 32 * 8192; }
        if (pass == 0) {
#pragma nounroll
          for (int s = 0; s < 7; ++s) { set_m0(l0 + L_X + s * 1024); dma1(lane * 16, x + s * 1024); }
        }
        asm volatile("s_waitcnt vmcnt(0)" : "+v"(o0), "+v"(o1), "+v"(mine)::"memory");
        f32x4 qa = {0.f, 0.f, 0.f, 0.f}, qb = {0.f, 0.f, 0.f, 0.f};
#pragma nounroll
        for (int s = 0; s < 14; s += 2) {
          const v2u a0 = ldtr(lp + s * 1024), a1 = ldtr(lp + s * 1024 + 128), b0 = ldtr(lp + s * 1024 + 1024), b1 = ldtr(lp + s * 1024 + 1152);
          qa = mfma32(v4u{a0.x, a0.y, a1.x, a1.y}, *(const LAS v4u*)(lx + s * 64), qa);
          qb = mfma32(v4u{b0.x, b0.y, b1.x, b1.y}, *(const LAS v4u*)(lx + s * 64 + 64), qb);
        }
        qa += qb;
        const v2u qq = {cvtpk(qa[0], qa[1]), cvtpk(qa[2], qa[3])};
        if (pass == 0) {
          STAMP(6);
          if (c < 8) __builtin_amdgcn_raw_buffer_store_b128(v4u{qq.x, mine.y + 1u, qq.y, mine.w + 1u}, rq, qout, 0, SC1);
          v4u v0, v1;
#pragma nounroll
          for (unsigned sp = 0;; ++sp) {
            __builtin_amdgcn_s_sleep(8);
            v0 = poll128(rq, qin);
            v1 = poll128(rq, qin + 1024);
            if (sp >= QSPIN || __all((v0.y != o0.y) & (v0.w != o0.w) & (v1.y != o1.y) & (v1.w != o1.w))) break;
          }
          *(LAS v2u*)(lds + L_Q + lane * 8) = v2u{v0.x, v0.z};
          *(LAS v2u*)(lds + L_Q + 512 + lane * 8) = v2u{v1.x, v1.z};
          miss0 = __ballot((v0.y == o0.y) | (v0.w == o0.w));
          miss1 = __ballot((v1.y == o1.y) | (v1.w == o1.w));
        } else {
          if (c == b) *(LAS v2u*)(lds + L_Q + (blk & 31) * 32 + g * 8) = qq;
        }
        if (miss0) { const int j = __builtin_ctzll(miss0) >> 2; miss0 &= ~(15ull << (4 * j)); blk = kvh * 32 + j; }
        else if (miss1) { const int j = __builtin_ctzll(miss1) >> 2; miss1 &= ~(15ull << (4 * j)); blk = kvh * 32 + 16 + j; }
        else break;
        waitlgkm();
      }
      waitlgkm();
      if (lane == 0) ctl[0] = 1u;
      STAMP(2);
    } else {
#pragma nounroll
      for (unsigned sp = 0; sp < LSPIN_CAP && ctl[0] == 0u; ++sp) __builtin_amdgcn_s_sleep(16);
    }
    if (hw == 1) {
      const unsigned long long clk = __builtin_amdgcn_s_memrealtime();
      const unsigned n0 = (unsigned)clk, n1 = (unsigned)(clk >> 8) & ~1u;
      const __amdgpu_buffer_rsrc_t rr = RSRC(ws + W_REQ + (size_t)bid * 256, 256);
      if (lane == 0) __builtin_amdgcn_raw_buffer_store_b64(v2u{n0, n1}, rr, 0, 0, SC1);
      v4u rep = {0u, 0u, 0u, 0u};
#pragma nounroll
      for (unsigned sp = 0; sp < SPIN_CAP; ++sp) {
        __builtin_amdgcn_s_sleep(64);
        rep = poll128(rr, 128);
        if (__builtin_amdgcn_readfirstlane(rep.y) == n0 && __builtin_amdgcn_readfirstlane(rep.w) == n1) break;
      }
      if (lane == 0) {
        __builtin_amdgcn_raw_buffer_store_b64(v2u{rep.x, rep.z}, rr, 0, 0, SC1);
        *(LAS v2u*)(lds + L_TAG) = v2u{rep.x, rep.z};
      }
      STAMP(2);
    } else if (hw == 2 && jb == 0) {
      const unsigned long long clk = __builtin_amdgcn_s_memrealtime();
      const unsigned t0 = (unsigned)clk, t1 = (unsigned)(clk >> 8) | 1u;
      const __amdgpu_buffer_rsrc_t rl = RSRC(ws + W_REQ + (size_t)bid * 256, 32 * 256);
      v2u last = {t0, t1};
#pragma nounroll
      for (unsigned sp = 0; sp < SPIN_CAP; ++sp) {
        __builtin_amdgcn_s_sleep(32);
        const v2u r = poll64(rl, (lane & 31) * 256);
        const bool d = (r.x == t0) & (r.y == t1);
        const bool plaus = (sp >= 64) | (r.x - t0 + 0x100000u < 0x200000u);
        if (lane < 32 && !d && plaus && (r.x != last.x || r.y != last.y)) {
          __builtin_amdgcn_raw_buffer_store_b128(v4u{t0, r.x, t1, r.y}, rl, lane * 256 + 128, 0, SC1);
          last = r;
        }
        if (__all(d)) break;
      }
    }
    const int ch = c < 14 ? c : 13;
    const char* wp = wo + (size_t)((2 * kvh + (g >> 1)) * 256 + 64 * m + 32 * (g & 1)) * (DM * 2) + hw * 224 + ch * 16;
    unsigned wr[16][8];
#pragma unroll
    for (int i0 = 0; i0 < 16; i0 += 2) {
      v4u r[4];
#pragma unroll
      for (int u = 0; u < 4; ++u) r[u] = *(const GAS v4u*)(wp + (size_t)(2 * i0 + u) * (DM * 2));
#pragma unroll
      for (int u = 0; u < 2; ++u) {
#pragma unroll
        for (int e = 0; e < 4; ++e) {
          wr[i0 + u][2 * e] = __builtin_amdgcn_perm(r[2 * u + 1][e], r[2 * u][e], 0x05040100u);
          wr[i0 + u][2 * e + 1] = __builtin_amdgcn_perm(r[2 * u + 1][e], r[2 * u][e], 0x07060302u);
        }
        asm volatile("" : "+v"(wr[i0 + u][0]), "+v"(wr[i0 + u][1]), "+v"(wr[i0 + u][2]), "+v"(wr[i0 + u][3]), "+v"(wr[i0 + u][4]), "+v"(wr[i0 + u][5]), "+v"(wr[i0 + u][6]), "+v"(wr[i0 + u][7])::"memory");
      }
    }
    if (hw == 3) {
      const __amdgpu_buffer_rsrc_t rc = RSRC(ws + W_CK, 64 * 4352);
#pragma nounroll
      for (int k = 0; k < CROUNDS; ++k) {
        const unsigned tgo = tk0 + CT0 + k * CSTEP;
        idle_until(tgo);
        if (bid % 17 == k && lane < 4) {
          const int l4 = ((int)ctl[2] + (int)ctl[3] + (int)ctl[4] + (int)ctl[5]) >> 2;
          __builtin_amdgcn_raw_buffer_store_b64(v2u{(unsigned)l4, tgo}, rc, (k * 4 + lane) * 4352 + (bid / 17) * 8, 0, SC1);
        }
        idle_until(tgo + CLAG);
        const v2u r = poll64(rc, (k * 4 + ((bid >> 3) & 3)) * 4352 + c * 8);
        const bool ok = (r.y - tgo + 500u < 1000u) & ((int)r.x > -20000) & ((int)r.x < 20000);
        float sum = ok ? (float)(int)r.x : 0.f, cnt = ok ? 1.f : 0.f;
#pragma unroll
        for (int o = 1; o < 16; o <<= 1) { sum += __shfl_xor(sum, o); cnt += __shfl_xor(cnt, o); }
        if (cnt >= 8.f && lane == 0) ctl[1] = (unsigned)(int)(sum / cnt);
      }
    }
    if (hw == 2) {
      idle_until(tk0 + 6400);
      const GAS char* cp = (const GAS char*)(pc0 & ~127ull) + lane * 128;
      const unsigned t0 = *(const volatile GAS unsigned*)cp, t1 = *(const volatile GAS unsigned*)(cp + CODE_HI);
      asm volatile("" ::"v"(t0), "v"(t1));
    }
    STAMP(1);
    bar();
    const v2u tag = *(const LAS v2u*)(lds + L_TAG);
    STAMP(3);
    bar();
    STAMP(4);
    float Mx, Ls, o0, o1;
    merge4(lds + L_RS + hd * 8, lds + L_RX + lane * 8, 512, Mx, Ls, o0, o1);
    const float inv = __builtin_amdgcn_rcpf(Ls);
    const unsigned op = cvtpk(o0 * inv, o1 * inv);
    float y[8];
#pragma unroll
    for (int e = 0; e < 8; ++e) y[e] = 0.f;
#define STEP(i) { const unsigned ob = row_bcast<i>(op); _Pragma("unroll") for (int e = 0; e < 8; ++e) y[e] = dot2(wr[i][e], ob, y[e]); }
    STEP(0) STEP(1) STEP(2) STEP(3) STEP(4) STEP(5) STEP(6) STEP(7) STEP(8) STEP(9) STEP(10) STEP(11) STEP(12) STEP(13) STEP(14) STEP(15)
#undef STEP
    float z[4];
#pragma unroll
    for (int e = 0; e < 4; ++e) { auto s = __builtin_amdgcn_permlane32_swap(asu(y[e]), asu(y[e + 4]), false, false); z[e] = asf(s[0]) + asf(s[1]); }
    float u[2];
#pragma unroll
    for (int e = 0; e < 2; ++e) { auto s = __builtin_amdgcn_permlane16_swap(asu(z[e]), asu(z[e + 2]), false, false); u[e] = asf(s[0]) + asf(s[1]); }
    if (c < 14) {
      const int n0 = hw * 112 + c * 8 + g * 2;
      const __amdgpu_buffer_rsrc_t ri = RSRC(ws + W_INB + (size_t)b * (32 * 8192), 32 * 8192);
      __builtin_amdgcn_raw_buffer_store_b128(v4u{asu(u[0]), tag.x, asu(u[1]), tag.y}, ri, (n0 >> 4) * 8192 + jb * 128 + (n0 & 15) * 8, 0, SC1);
    }
    STAMP(5);
  }
  STAMP_FLUSH();
}

extern "C" void kernel_launch(void* const* d_in, const int* in_sizes, int n_in, void* d_out, int out_size, void* d_ws, size_t ws_size, hipStream_t stream) {
  (void)hipFuncSetAttribute((const void*)k_main, hipFuncAttributeMaxDynamicSharedMemorySize, L_END);
  k_main<<<256, 512, L_END, stream>>>((const char*)d_in[0], (const char*)d_in[1], (const char*)d_in[2], (const char*)d_in[3], (const char*)d_in[4], (char*)d_ws, (unsigned*)d_out);
}
```

```cpp
#define STAMPS
#include <hip/hip_runtime.h>
#include <hip/hip_bf16.h>
#include <type_traits>

typedef unsigned v4u __attribute__((ext_vector_type(4)));
typedef unsigned v2u __attribute__((ext_vector_type(2)));
typedef float f32x4 __attribute__((ext_vector_type(4)));
typedef float f32x2 __attribute__((ext_vector_type(2)));
typedef short bf16x8 __attribute__((ext_vector_type(8)));
typedef short s16x4 __attribute__((ext_vector_type(4)));
typedef __bf16 bf16x2 __attribute__((ext_vector_type(2)));
#define LAS __attribute__((address_space(3)))
#define GAS __attribute__((address_space(1)))

constexpr int DM = 448;
constexpr float C2 = 0.0625f * 1.4426950408889634f;
constexpr int RS = 4;
constexpr int RH = 4;
#define WLO 4
#define WMID 6
#define WHI 8
#define DUTY 8
#define TAU 1080
#define TOFF 0
#define PG 12
#define QSPIN 16
#define CROUNDS 4
#define CT0 2000
#define CSTEP 1200
#define CLAG 250
#define CODE_HI 4480
constexpr unsigned SPIN_CAP = 1u << 18, LSPIN_CAP = 1u << 24;

constexpr size_t W_REQ = 0;
constexpr size_t W_QG = 65536;
constexpr size_t W_INB = W_QG + 131072;
constexpr size_t W_CK = W_INB + 256 * 8192;
constexpr size_t W_TS = 8u << 20;

constexpr int L_RING = 0;
constexpr int L_SC = 4 * RS * 8192;
constexpr int L_Q = L_SC + 16384;
constexpr int L_ST = L_Q + 1024;
constexpr int L_RX = L_ST + 64;
constexpr int L_RS = L_RX + 2048;
constexpr int L_TAG = L_RS + 64;
constexpr int L_X = L_TAG + 64;
constexpr int L_END = L_X + 7168;

template <int N> __device__ __forceinline__ void waitvm() { asm volatile("s_waitcnt vmcnt(%0)" ::"i"(N) : "memory"); }
__device__ __forceinline__ void waitlgkm() { asm volatile("s_waitcnt lgkmcnt(0)" ::: "memory"); }
__device__ __forceinline__ void bar() { asm volatile("s_waitcnt lgkmcnt(0)\n\ts_barrier" ::: "memory"); }
__device__ __forceinline__ unsigned now() { return (unsigned)__builtin_amdgcn_s_memrealtime(); }
__device__ __forceinline__ void set_m0(unsigned v) { asm volatile("s_mov_b32 m0, %0\n\ts_nop 0" ::"s"(v) : "memory"); }
__device__ __forceinline__ void dma1(unsigned voff, const void* sbase) { asm volatile("global_load_lds_dwordx4 %0, %1" ::"v"(voff), "s"(sbase) : "memory"); }
#define BR3(p) ((((p) & 1) << 2) | ((p) & 2) | (((p) >> 2) & 1))
#define WMASK(d) ((1u << (d)) - 1u)
template <bool WAIT, int P, int OFF> __device__ __forceinline__ void piece(unsigned wm, unsigned voff, const void* sbase) {
  if constexpr (WAIT)
    asm volatile(
        "s_waitcnt vmcnt(%5)\n\t"
        "s_bitcmp1_b32 %0, %7\n\t"
        "s_cbranch_scc1 1f\n\t"
        "s_waitcnt vmcnt(%4)\n\t"
        "s_bitcmp1_b32 %0, %6\n\t"
        "s_cbranch_scc1 1f\n\t"
        "s_waitcnt vmcnt(%3)\n"
        "1:\n\t"
        "global_load_lds_dwordx4 %1, %2 offset:%8 nt" ::"s"(wm), "v"(voff), "s"(sbase), "i"(WLO - 1), "i"(WMID - 1), "i"(WHI - 1), "i"(BR3(P)), "i"(BR3(P) + 8), "i"(OFF)
        : "memory", "scc");
  else
    asm volatile("global_load_lds_dwordx4 %0, %1 offset:%2 nt" ::"v"(voff), "s"(sbase), "i"(OFF) : "memory");
}
__device__ __forceinline__ f32x4 mfma32(v4u a, v4u b, f32x4 c) {
  return __builtin_amdgcn_mfma_f32_16x16x32_bf16(__builtin_bit_cast(bf16x8, a), __builtin_bit_cast(bf16x8, b), c, 0, 0, 0);
}
__device__ __forceinline__ f32x4 mfma16(v2u a, v2u b, f32x4 c) {
  return __builtin_amdgcn_mfma_f32_16x16x16bf16_1k(__builtin_bit_cast(s16x4, a), __builtin_bit_cast(s16x4, b), c, 0, 0, 0);
}
__device__ __forceinline__ v2u ldtr(const LAS char* p) { return __builtin_bit_cast(v2u, __builtin_amdgcn_ds_read_tr16_b64_v4i16((LAS s16x4*)p)); }
__device__ __forceinline__ unsigned cvtpk(float lo, float hi) { return __builtin_bit_cast(unsigned, __builtin_convertvector(f32x2{lo, hi}, bf16x2)); }
__device__ __forceinline__ float dot2(unsigned a, unsigned b, float acc) {
  return __builtin_amdgcn_fdot2_f32_bf16(__builtin_bit_cast(bf16x2, a), __builtin_bit_cast(bf16x2, b), acc, false);
}
__device__ __forceinline__ float asf(unsigned u) { return __builtin_bit_cast(float, u); }
__device__ __forceinline__ unsigned asu(float f) { return __builtin_bit_cast(unsigned, f); }
__device__ __forceinline__ float ex2(float x) { return __builtin_amdgcn_exp2f(x); }
__device__ __forceinline__ float rows_max(float x) {
  auto s = __builtin_amdgcn_permlane16_swap(asu(x), asu(x), false, false);
  x = fmaxf(asf(s[0]), asf(s[1]));
  auto t = __builtin_amdgcn_permlane32_swap(asu(x), asu(x), false, false);
  return fmaxf(asf(t[0]), asf(t[1]));
}
__device__ __forceinline__ float rows_sum(float x) {
  auto s = __builtin_amdgcn_permlane16_swap(asu(x), asu(x), false, false);
  x = asf(s[0]) + asf(s[1]);
  auto t = __builtin_amdgcn_permlane32_swap(asu(x), asu(x), false, false);
  return asf(t[0]) + asf(t[1]);
}
template <int N> __device__ __forceinline__ unsigned row_bcast(unsigned x) { return (unsigned)__builtin_amdgcn_mov_dpp((int)x, 0x150 + N, 0xf, 0xf, true); }
#define RSRC(p, n) __builtin_amdgcn_make_buffer_rsrc((void*)(p), 0, (int)(n), 0x00020000)
constexpr int SC1 = 16;
__device__ __forceinline__ v4u poll128(__amdgpu_buffer_rsrc_t rs, int off) {
  asm volatile("" : "+v"(off)::"memory");
  return __builtin_amdgcn_raw_buffer_load_b128(rs, off, 0, SC1);
}
__device__ __forceinline__ v2u poll64(__amdgpu_buffer_rsrc_t rs, int off) {
  asm volatile("" : "+v"(off)::"memory");
  return __builtin_amdgcn_raw_buffer_load_b64(rs, off, 0, SC1);
}
__device__ __forceinline__ void idle_until(unsigned t) {
#pragma nounroll
  for (unsigned sp = 0; sp < 4096u && (int)(now() - t) < 0; ++sp) __builtin_amdgcn_s_sleep(16);
}
#define PINV(x) asm volatile("" : "+v"(x))
#define STAMP(i) do { tsv[i] = __builtin_amdgcn_s_memrealtime(); asm volatile("s_waitcnt lgkmcnt(0)" : "+s"(tsv[i]) :: "memory"); } while (0)
#define STAMP_FLUSH() do { if (lane == 0) { _Pragma("unroll") for (int i_ = 0; i_ < 8; ++i_) ((unsigned long long*)(ws + W_TS))[(blockIdx.x * 8 + wave) * 8 + i_] = tsv[i_]; } } while (0)

__device__ __forceinline__ void merge4(const LAS char* stp, const LAS char* pap, int pstride, float& Mx, float& Ls, float& o0, float& o1) {
  f32x2 st[4], pa[4];
#pragma unroll
  for (int w = 0; w < 4; ++w) {
    st[w] = *(const LAS f32x2*)(stp + w * 16);
    pa[w] = *(const LAS f32x2*)(pap + w * pstride);
  }
  Mx = fmaxf(fmaxf(st[0].x, st[1].x), fmaxf(st[2].x, st[3].x));
  Ls = 0.f; o0 = 0.f; o1 = 0.f;
#pragma unroll
  for (int w = 0; w < 4; ++w) {
    const float f = ex2(st[w].x - Mx);
    Ls = fmaf(f, st[w].y, Ls); o0 = fmaf(f, pa[w].x, o0); o1 = fmaf(f, pa[w].y, o1);
  }
}

__global__ void __launch_bounds__(512, 2) k_main(const char* x, const char* Kc, const char* Vc, const char* wq, const char* wo, char* ws, unsigned* out) {
  extern __shared__ __attribute__((aligned(16))) char lds_[];
  LAS char* lds = (LAS char*)lds_;
  const int tid = threadIdx.x, lane = tid & 63, wave = __builtin_amdgcn_readfirstlane(tid >> 6);
  const int bid = blockIdx.x, grp = bid >> 2, m = bid & 3, b = bid >> 5, jb = bid & 31, kvh = jb >> 2;
  const int g = lane >> 4, c = lane & 15, hd = lane >> 5;
  unsigned long long tsv[8] = {0, 0, 0, 0, 0, 0, 0, 0};
  asm volatile("" : "+s"(x), "+s"(Kc), "+s"(Vc), "+s"(wq), "+s"(wo), "+s"(ws), "+s"(out));
  unsigned long long pc0;
  asm volatile("s_getpc_b64 %0" : "=s"(pc0));
  STAMP(0);
  volatile LAS unsigned* ctl = (volatile LAS unsigned*)(lds + L_TAG + 8);
  if (tid < 6) ctl[tid] = 0u;
  if (__builtin_expect(wave < 4, 1)) {
    __builtin_amdgcn_s_setprio(3);
    const size_t t0 = (size_t)(bid >> 1) * 2097152 + (bid & 1) * 32768 + wave * 8192;
    const char* src = Kc + t0;
    const unsigned ring0 = (unsigned)(size_t)lds + L_RING + wave * (RS * 8192);
    unsigned vo[8];
#pragma unroll
    for (int p = 0; p < 8; ++p) vo[p] = hd * 512 + ((((lane & 31) ^ hd) * 16) ^ (p << 5)) + (p >> 2) * 4096;
    unsigned wm = WMASK(DUTY);
#define ISW(W, p) do { if (((p) & 3) == 0) set_m0(dst + ((p) >> 2) * 4096); piece<W, (p), ((p) & 3) * 1024>(wm, vo[p], src); } while (0)
#define ISS(p) ISW(true, p)
    {
      const unsigned dst = ring0;
      ISW(false, 0); ISW(false, 1); ISW(false, 2); ISW(false, 3); ISW(false, 4); ISW(false, 5); ISW(false, 6); ISW(false, 7);
      src += 65536;
    }
    unsigned sched = now() * 10u + (unsigned)(TOFF * 10 + (RH + RS) * TAU);
    unsigned tnow = 0, adj = 0;
    bar();
#define PACE_STEP() do { tnow = now(); adj = ctl[1]; } while (0)
#define PACE_TAKE() do { const int late_ = (int)(tnow * 10u - sched); if (lane == 0) ctl[2 + wave] = (unsigned)late_; \
      int d_ = DUTY + (((late_ - (int)adj) * PG) / 1280); d_ = d_ < 0 ? 0 : d_ > 16 ? 16 : d_; wm = __builtin_amdgcn_readfirstlane(WMASK(d_)); sched += TAU; } while (0)
    const LAS char* wring = lds + L_RING + wave * (RS * 8192);
    LAS char* scw = lds + L_SC + wave * 4096 + (g * 2 + (c & 1)) * 16;
    const int kro = c * 512 + ((g ^ c) * 16);
#define KFRAG(base_, p) (*(const LAS v4u*)((base_) + ((kro ^ (((p) & 3) << 6)) + ((p) >> 2) * 256)))
    v4u hold[RH][8];
#pragma nounroll
    for (int r = 1;; ++r) {
      const int sl = r >= RS ? r - RS : r;
      if (r >= RS) {
        waitvm<8 * (RS - 1)>();
#pragma unroll
        for (int j = 0; j < RH; ++j)
          if (sl == j) {
#pragma unroll
            for (int p = 0; p < 8; ++p) hold[j][p] = KFRAG(wring + j * 8192, p);
          }
        waitlgkm();
        if (r == RS + RH - 1) break;
      }
      const unsigned dst = ring0 + sl * 8192;
      ISS(0); ISS(1); ISS(2); ISS(3); ISS(4); ISS(5); ISS(6); ISS(7);
      src += 65536;
    }
    STAMP(6);
#pragma nounroll
    for (unsigned sp = 0; sp < LSPIN_CAP && ctl[0] == 0u; ++sp) __builtin_amdgcn_s_sleep(1);
    asm volatile("" ::: "memory");
    STAMP(1);
    v4u qf[8];
    float mx = -INFINITY;
    f32x4 acc[16], accl;
    int fs = RH - 1, cs = RH % RS;
#define HOLD(r) do { if (FIRST) { f32x4 h = {0.f, 0.f, 0.f, 0.f}; _Pragma("unroll") for (int p = 0; p < 8; ++p) h = mfma32(hold[r][p], qf[p], h); \
      if (c < 2) *(LAS f32x4*)(scw + (r) * 128) = h; mx = fmaxf(fmaxf(mx, h[0]), fmaxf(fmaxf(h[1], h[2]), h[3])); PINV(mx); } } while (0)
    auto step_a = [&](auto first, int s) {
      constexpr bool FIRST = decltype(first)::value;
      waitvm<8 * (RS - 2)>();
      const LAS char* base = wring + cs * 8192;
      const unsigned dst = ring0 + fs * 8192;
      ISS(0);
      if (FIRST) {
#pragma unroll
        for (int p = 0; p < 8; ++p) qf[p] = *(const LAS v4u*)(lds + L_Q + (c & 1) * 512 + p * 64 + g * 16);
      }
      v4u kf[8];
#pragma unroll
      for (int p = 0; p < 8; ++p) kf[p] = KFRAG(base, p);
      f32x4 a = {0.f, 0.f, 0.f, 0.f};
      ISS(1);
      a = mfma32(kf[0], qf[0], a); a = mfma32(kf[1], qf[1], a); PINV(a);
      ISS(2);
      a = mfma32(kf[2], qf[2], a); a = mfma32(kf[3], qf[3], a); PINV(a);
      HOLD(0);
      ISS(3);
      a = mfma32(kf[4], qf[4], a); a = mfma32(kf[5], qf[5], a); PINV(a);
      HOLD(1);
      ISS(4);
      a = mfma32(kf[6], qf[6], a); a = mfma32(kf[7], qf[7], a); PINV(a);
      HOLD(2);
      ISS(5);
      if (c < 2) *(LAS f32x4*)(scw + s * 128) = a;
      mx = fmaxf(fmaxf(mx, a[0]), fmaxf(fmaxf(a[1], a[2]), a[3])); PINV(mx);
      HOLD(3);
      ISS(6);
      if (FIRST) {
        accl = f32x4{0.f, 0.f, 0.f, 0.f}; PINV(accl);
#pragma unroll
        for (int d = 0; d < 16; ++d) { acc[d] = f32x4{0.f, 0.f, 0.f, 0.f}; PINV(acc[d]); }
      }
      PACE_STEP();
      ISS(7);
      PACE_TAKE();
      src += 65536;
      fs = cs; cs = (cs == RS - 1) ? 0 : cs + 1;
    };
    step_a(std::true_type{}, RH);
#pragma nounroll
    for (int s = RH + 1; s < 32; ++s) {
      if (s == 33 - RS) {
        src = Vc + t0;
#pragma unroll
        for (int p = 0; p < 8; ++p) vo[p] = hd * 512 + ((((lane & 31) ^ (hd << 1)) * 16) ^ ((p & 3) << 6)) + (p >> 2) * 4096;
      }
      step_a(std::false_type{}, s);
    }
    const float M = rows_max(mx) * C2;
    STAMP(2);
    const int kq = 4 * g + ((lane >> 2) & 3);
    const int vro = kq * 512 + (kq & 7) * 32 + (lane & 3) * 8;
    const v2u ones = {0x3F803F80u, 0x3F803F80u};
#define PFRAG(sc_) v2u{cvtpk(ex2(fmaf(sc_[0], C2, -M)), ex2(fmaf(sc_[1], C2, -M))), cvtpk(ex2(fmaf(sc_[2], C2, -M)), ex2(fmaf(sc_[3], C2, -M)))}
#define PVM(p) do { acc[p] = mfma16(vf[p], pf, acc[p]); acc[(p) + 8] = mfma16(vf[(p) + 8], pf, acc[(p) + 8]); asm volatile("" : "+v"(acc[p]), "+v"(acc[(p) + 8])); } while (0)
#pragma nounroll
    for (int i = 0; i < 33 - RS; ++i) {
      waitvm<8 * (RS - 2)>();
      const LAS char* base = wring + cs * 8192;
      const unsigned dst = ring0 + fs * 8192;
      ISS(0);
      const f32x4 sc = *(const LAS f32x4*)(scw + i * 128);
      v2u vf[16];
#pragma unroll
      for (int p = 0; p < 4; ++p) { const LAS char* a = base + (vro ^ (p << 5)); vf[p] = ldtr(a); vf[p + 8] = ldtr(a + 256); }
      ISS(1);
#pragma unroll
      for (int p = 4; p < 8; ++p) { const LAS char* a = base + (vro ^ (p << 5)); vf[p] = ldtr(a); vf[p + 8] = ldtr(a + 256); }
      v2u pf = PFRAG(sc);
      PINV(pf);
      ISS(2);
      accl = mfma16(ones, pf, accl);
      PVM(0); PVM(1);
      ISS(3);
      PVM(2); PVM(3);
      ISS(4);
      PVM(4); PVM(5);
      ISS(5);
      PVM(6); PVM(7);
      ISS(6);
      PACE_STEP();
      ISS(7);
      PACE_TAKE();
      src += 65536;
      fs = cs; cs = (cs == RS - 1) ? 0 : cs + 1;
    }
#pragma nounroll
    for (int i = 33 - RS; i < 32; ++i) {
      const LAS char* base = wring + cs * 8192;
      const f32x4 sc = *(const LAS f32x4*)(scw + i * 128);
      v2u pf = PFRAG(sc);
      accl = mfma16(ones, pf, accl);
      asm volatile("" : "+v"(pf), "+v"(accl));
      if (i == 31) waitvm<0>(); else waitvm<8>();
#pragma unroll
      for (int p = 0; p < 8; ++p) {
        const LAS char* a = base + (vro ^ (p << 5));
        acc[p] = mfma16(ldtr(a), pf, acc[p]);
        acc[p + 8] = mfma16(ldtr(a + 256), pf, acc[p + 8]);
      }
      cs = (cs == RS - 1) ? 0 : cs + 1;
    }
    waitlgkm();
    if (c < 2) {
#pragma unroll
      for (int d = 0; d < 16; ++d) *(LAS f32x4*)(lds + L_RING + wave * (RS * 8192) + c * 1024 + (16 * d + 4 * g) * 4) = acc[d];
      if (g == 0) *(LAS f32x2*)(lds + L_ST + wave * 16 + c * 8) = f32x2{M, accl[0]};
    }
    STAMP(3);
    bar();
    const v2u tag = *(const LAS v2u*)(lds + L_TAG);
    {
      float Mx, Ls, o0, o1;
      merge4(lds + L_ST + hd * 8, lds + L_RING + hd * 1024 + wave * 256 + (lane & 31) * 8, RS * 8192, Mx, Ls, o0, o1);
      if (wave != m) {
        const __amdgpu_buffer_rsrc_t ro = RSRC(ws + W_INB + (size_t)(grp * 4 + wave) * 8192 + 4096 + (((m - wave) & 3) - 1) * 1152, 1152);
        __builtin_amdgcn_raw_buffer_store_b128(v4u{asu(o0), tag.x, asu(o1), tag.y}, ro, lane * 16, 0, SC1);
        if ((lane & 31) == 0) __builtin_amdgcn_raw_buffer_store_b128(v4u{asu(Mx), tag.x, asu(Ls), tag.y}, ro, 1024 + hd * 16, 0, SC1);
        STAMP(4);
        const __amdgpu_buffer_rsrc_t ri = RSRC(ws + W_INB + (size_t)bid * 8192 + 4096 + (((wave - m) & 3) - 1) * 1152, 1152);
        v4u v, s;
#pragma nounroll
        for (unsigned sp = 0; sp < SPIN_CAP; ++sp) {
          v = poll128(ri, lane * 16);
          s = poll128(ri, 1024 + hd * 16);
          if (__all((v.y == tag.x) & (v.w == tag.y) & (s.y == tag.x) & (s.w == tag.y))) break;
        }
        o0 = asf(v.x); o1 = asf(v.z); Mx = asf(s.x); Ls = asf(s.z);
      } else {
        const v2u u = poll64(RSRC(ws + W_INB + (size_t)bid * 8192, 4096), lane * 64);
        asm volatile("" ::"v"(u));
      }
      *(LAS f32x2*)(lds + L_RX + wave * 512 + lane * 8) = f32x2{o0, o1};
      if ((lane & 31) == 0) *(LAS f32x2*)(lds + L_RS + wave * 16 + hd * 8) = f32x2{Mx, Ls};
    }
    bar();
    if (wave == 0 && jb < 28) {
      const __amdgpu_buffer_rsrc_t rs = RSRC(ws + W_INB + (size_t)bid * 8192, 4096);
      v4u v[4];
      __builtin_amdgcn_s_sleep(12);
#pragma nounroll
      for (unsigned sp = 0; sp < SPIN_CAP; ++sp) {
#pragma unroll
        for (int it = 0; it < 4; ++it) v[it] = poll128(rs, it * 1024 + lane * 16);
        bool ok = true;
#pragma unroll
        for (int it = 0; it < 4; ++it) ok &= (v[it].y == tag.x) & (v[it].w == tag.y);
        if (__all(ok)) break;
      }
      float s0 = (asf(v[0].x) + asf(v[1].x)) + (asf(v[2].x) + asf(v[3].x));
      float s1 = (asf(v[0].z) + asf(v[1].z)) + (asf(v[2].z) + asf(v[3].z));
      s0 += asf(__builtin_amdgcn_mov_dpp((int)asu(s0), 0x128, 0xf, 0xf, true));
      s1 += asf(__builtin_amdgcn_mov_dpp((int)asu(s1), 0x128, 0xf, 0xf, true));
      s0 = rows_sum(s0); s1 = rows_sum(s1);
      if (lane < 8) __builtin_amdgcn_raw_buffer_store_b32(cvtpk(s0, s1), RSRC(out, 8 * DM * 2), (b * (DM / 2) + jb * 8 + lane) * 4, 0, 17);
      STAMP(5);
    }
  } else {
    const int hw = wave - 4;
    bar();
    const unsigned tk0 = now();
    if (hw == 0) {
      const int cb = (bid & 7) * 32 + (bid >> 3);
      const __amdgpu_buffer_rsrc_t rq = RSRC(ws + W_QG, 131072);
      const int qin = b * 16384 + kvh * 2048 + lane * 16, qout = (c & 7) * 16384 + (cb * 4 + g) * 16;
      v4u o0 = poll128(rq, qin), o1 = poll128(rq, qin + 1024), mine = poll128(rq, qout);
      const unsigned l0 = (unsigned)(size_t)lds;
      const unsigned voff = (lane >> 1) * 8192 + (lane & 1) * 16;
      const LAS char* lp = lds + L_SC + (8 * g + ((lane >> 2) & 3)) * 32 + (lane & 3) * 8;
      const LAS char* lx = lds + L_X + (c & 7) * (DM * 2) + g * 16;
      unsigned long long miss0 = 0, miss1 = 0;
      int blk = cb;
#pragma nounroll
      for (int pass = 0;; ++pass) {
        const char* wsrc = wq + blk * 32;
#pragma nounroll
        for (int s = 0; s < 14; ++s) { set_m0(l0 + L_SC + s * 1024); dma1(voff, wsrc); wsrc += 32 * 8192; }
        if (pass == 0) {
#pragma nounroll
          for (int s = 0; s < 7; ++s) { set_m0(l0 + L_X + s * 1024); dma1(lane * 16, x + s * 1024); }
        }
        asm volatile("s_waitcnt vmcnt(0)" : "+v"(o0), "+v"(o1), "+v"(mine)::"memory");
        f32x4 qa = {0.f, 0.f, 0.f, 0.f}, qb = {0.f, 0.f, 0.f, 0.f};
#pragma nounroll
        for (int s = 0; s < 14; s += 2) {
          const v2u a0 = ldtr(lp + s * 1024), a1 = ldtr(lp + s * 1024 + 128), b0 = ldtr(lp + s * 1024 + 1024), b1 = ldtr(lp + s * 1024 + 1152);
          qa = mfma32(v4u{a0.x, a0.y, a1.x, a1.y}, *(const LAS v4u*)(lx + s * 64), qa);
          qb = mfma32(v4u{b0.x, b0.y, b1.x, b1.y}, *(const LAS v4u*)(lx + s * 64 + 64), qb);
        }
        qa += qb;
        const v2u qq = {cvtpk(qa[0], qa[1]), cvtpk(qa[2], qa[3])};
        if (pass == 0) {
          STAMP(6);
          if (c < 8) __builtin_amdgcn_raw_buffer_store_b128(v4u{qq.x, mine.y + 1u, qq.y, mine.w + 1u}, rq, qout, 0, SC1);
          v4u v0, v1;
#pragma nounroll
          for (unsigned sp = 0;; ++sp) {
            __builtin_amdgcn_s_sleep(8);
            v0 = poll128(rq, qin);
            v1 = poll128(rq, qin + 1024);
            if (sp >= QSPIN || __all((v0.y != o0.y) & (v0.w != o0.w) & (v1.y != o1.y) & (v1.w != o1.w))) break;
          }
          *(LAS v2u*)(lds + L_Q + lane * 8) = v2u{v0.x, v0.z};
          *(LAS v2u*)(lds + L_Q + 512 + lane * 8) = v2u{v1.x, v1.z};
          miss0 = __ballot((v0.y == o0.y) | (v0.w == o0.w));
          miss1 = __ballot((v1.y == o1.y) | (v1.w == o1.w));
        } else {
          if (c == b) *(LAS v2u*)(lds + L_Q + (blk & 31) * 32 + g * 8) = qq;
        }
        if (miss0) { const int j = __builtin_ctzll(miss0) >> 2; miss0 &= ~(15ull << (4 * j)); blk = kvh * 32 + j; }
        else if (miss1) { const int j = __builtin_ctzll(miss1) >> 2; miss1 &= ~(15ull << (4 * j)); blk = kvh * 32 + 16 + j; }
        else break;
        waitlgkm();
      }
      waitlgkm();
      if (lane == 0) ctl[0] = 1u;
      STAMP(2);
    } else {
#pragma nounroll
      for (unsigned sp = 0; sp < LSPIN_CAP && ctl[0] == 0u; ++sp) __builtin_amdgcn_s_sleep(16);
    }
    if (hw == 1) {
      const unsigned long long clk = __builtin_amdgcn_s_memrealtime();
      const unsigned n0 = (unsigned)clk, n1 = (unsigned)(clk >> 8) & ~1u;
      const __amdgpu_buffer_rsrc_t rr = RSRC(ws + W_REQ + (size_t)bid * 256, 256);
      if (lane == 0) __builtin_amdgcn_raw_buffer_store_b64(v2u{n0, n1}, rr, 0, 0, SC1);
      v4u rep = {0u, 0u, 0u, 0u};
#pragma nounroll
      for (unsigned sp = 0; sp < SPIN_CAP; ++sp) {
        __builtin_amdgcn_s_sleep(64);
        rep = poll128(rr, 128);
        if (__builtin_amdgcn_readfirstlane(rep.y) == n0 && __builtin_amdgcn_readfirstlane(rep.w) == n1) break;
      }
      if (lane == 0) {
        __builtin_amdgcn_raw_buffer_store_b64(v2u{rep.x, rep.z}, rr, 0, 0, SC1);
        *(LAS v2u*)(lds + L_TAG) = v2u{rep.x, rep.z};
      }
      STAMP(2);
    } else if (hw == 2 && jb == 0) {
      const unsigned long long clk = __builtin_amdgcn_s_memrealtime();
      const unsigned t0 = (unsigned)clk, t1 = (unsigned)(clk >> 8) | 1u;
      const __amdgpu_buffer_rsrc_t rl = RSRC(ws + W_REQ + (size_t)bid * 256, 32 * 256);
      v2u last = {t0, t1};
#pragma nounroll
      for (unsigned sp = 0; sp < SPIN_CAP; ++sp) {
        __builtin_amdgcn_s_sleep(32);
        const v2u r = poll64(rl, (lane & 31) * 256);
        const bool d = (r.x == t0) & (r.y == t1);
        const bool plaus = (sp >= 64) | (r.x - t0 + 0x100000u < 0x200000u);
        if (lane < 32 && !d && plaus && (r.x != last.x || r.y != last.y)) {
          __builtin_amdgcn_raw_buffer_store_b128(v4u{t0, r.x, t1, r.y}, rl, lane * 256 + 128, 0, SC1);
          last = r;
        }
        if (__all(d)) break;
      }
    }
    const int ch = c < 14 ? c : 13;
    const char* wp = wo + (size_t)((2 * kvh + (g >> 1)) * 256 + 64 * m + 32 * (g & 1)) * (DM * 2) + hw * 224 + ch * 16;
    unsigned wr[16][8];
#pragma unroll
    for (int i0 = 0; i0 < 16; i0 += 2) {
      v4u r[4];
#pragma unroll
      for (int u = 0; u < 4; ++u) r[u] = *(const GAS v4u*)(wp + (size_t)(2 * i0 + u) * (DM * 2));
#pragma unroll
      for (int u = 0; u < 2; ++u) {
#pragma unroll
        for (int e = 0; e < 4; ++e) {
          wr[i0 + u][2 * e] = __builtin_amdgcn_perm(r[2 * u + 1][e], r[2 * u][e], 0x05040100u);
          wr[i0 + u][2 * e + 1] = __builtin_amdgcn_perm(r[2 * u + 1][e], r[2 * u][e], 0x07060302u);
        }
        asm volatile("" : "+v"(wr[i0 + u][0]), "+v"(wr[i0 + u][1]), "+v"(wr[i0 + u][2]), "+v"(wr[i0 + u][3]), "+v"(wr[i0 + u][4]), "+v"(wr[i0 + u][5]), "+v"(wr[i0 + u][6]), "+v"(wr[i0 + u][7])::"memory");
      }
    }
    if (hw == 3) {
      const __amdgpu_buffer_rsrc_t rc = RSRC(ws + W_CK, 64 * 4352);
#pragma nounroll
      for (int k = 0; k < CROUNDS; ++k) {
        const unsigned tgo = tk0 + CT0 + k * CSTEP;
        idle_until(tgo);
        if (bid % 17 == k && lane < 4) {
          const int l4 = ((int)ctl[2] + (int)ctl[3] + (int)ctl[4] + (int)ctl[5]) >> 2;
          __builtin_amdgcn_raw_buffer_store_b64(v2u{(unsigned)l4, tgo}, rc, (k * 4 + lane) * 4352 + (bid / 17) * 8, 0, SC1);
        }
        idle_until(tgo + CLAG);
        const v2u r = poll64(rc, (k * 4 + ((bid >> 3) & 3)) * 4352 + c * 8);
        const bool ok = (r.y - tgo + 500u < 1000u) & ((int)r.x > -20000) & ((int)r.x < 20000);
        float sum = ok ? (float)(int)r.x : 0.f, cnt = ok ? 1.f : 0.f;
#pragma unroll
        for (int o = 1; o < 16; o <<= 1) { sum += __shfl_xor(sum, o); cnt += __shfl_xor(cnt, o); }
        if (cnt >= 8.f && lane == 0) ctl[1] = (unsigned)(int)(sum / cnt);
      }
    }
    if (hw == 2) {
      idle_until(tk0 + 6400);
      const GAS char* cp = (const GAS char*)(pc0 & ~127ull) + lane * 128;
      const unsigned t0 = *(const volatile GAS unsigned*)cp, t1 = *(const volatile GAS unsigned*)(cp + CODE_HI);
      asm volatile("" ::"v"(t0), "v"(t1));
    }
    STAMP(1);
    bar();
    const v2u tag = *(const LAS v2u*)(lds + L_TAG);
    STAMP(3);
    bar();
    STAMP(4);
    float Mx, Ls, o0, o1;
    merge4(lds + L_RS + hd * 8, lds + L_RX + lane * 8, 512, Mx, Ls, o0, o1);
    const float inv = __builtin_amdgcn_rcpf(Ls);
    const unsigned op = cvtpk(o0 * inv, o1 * inv);
    float y[8];
#pragma unroll
    for (int e = 0; e < 8; ++e) y[e] = 0.f;
#define STEP(i) { const unsigned ob = row_bcast<i>(op); _Pragma("unroll") for (int e = 0; e < 8; ++e) y[e] = dot2(wr[i][e], ob, y[e]); }
    STEP(0) STEP(1) STEP(2) STEP(3) STEP(4) STEP(5) STEP(6) STEP(7) STEP(8) STEP(9) STEP(10) STEP(11) STEP(12) STEP(13) STEP(14) STEP(15)
#undef STEP
    float z[4];
#pragma unroll
    for (int e = 0; e < 4; ++e) { auto s = __builtin_amdgcn_permlane32_swap(asu(y[e]), asu(y[e + 4]), false, false); z[e] = asf(s[0]) + asf(s[1]); }
    float u[2];
#pragma unroll
    for (int e = 0; e < 2; ++e) { auto s = __builtin_amdgcn_permlane16_swap(asu(z[e]), asu(z[e + 2]), false, false); u[e] = asf(s[0]) + asf(s[1]); }
    if (c < 14) {
      const int n0 = hw * 112 + c * 8 + g * 2;
      const __amdgpu_buffer_rsrc_t ri = RSRC(ws + W_INB + (size_t)b * (32 * 8192), 32 * 8192);
      __builtin_amdgcn_raw_buffer_store_b128(v4u{asu(u[0]), tag.x, asu(u[1]), tag.y}, ri, (n0 >> 4) * 8192 + jb * 128 + (n0 & 15) * 8, 0, SC1);
    }
    STAMP(5);
  }
  STAMP_FLUSH();
}

__global__ void k_rep(const char* ws) {
  __shared__ unsigned long long sm[12][256];
  const unsigned long long tr0 = __builtin_amdgcn_s_memrealtime();
  const unsigned long long* ts = (const unsigned long long*)(ws + W_TS);
  const int g = threadIdx.x;
  unsigned long long mn = ~0ull, mx = 0;
  for (int w = 0; w < 8; ++w) { unsigned long long t = ts[(g * 8 + w) * 8]; mn = t < mn ? t : mn; for (int i = 0; i < 8; ++i) { t = ts[(g * 8 + w) * 8 + i]; mx = t > mx ? t : mx; } }
  sm[0][g] = mn; sm[1][g] = mx;
  for (int i = 1; i < 8; ++i) sm[1 + i][g] = ts[(g * 8 + 0) * 8 + i];
  sm[9][g] = ts[(g * 8 + 4) * 8 + 2]; sm[10][g] = ts[(g * 8 + 4) * 8 + 6];
  __syncthreads();
  if (g == 0) {
    for (int i = 0; i < 256; ++i) { mn = sm[0][i] < mn ? sm[0][i] : mn; mx = sm[1][i] > mx ? sm[1][i] : mx; }
    unsigned long long av[12] = {0}, ma[12] = {0};
    for (int r = 2; r < 11; ++r) for (int i = 0; i < 256; ++i) { unsigned long long t = sm[r][i] > mn ? sm[r][i] - mn : 0; av[r] += t; ma[r] = t > ma[r] ? t : ma[r]; }
    unsigned long long D = ma[9];
    if (D > 20000) D = 20000;
    const unsigned long long tend = mn + 20000 + D * 20;
    while (__builtin_amdgcn_s_memrealtime() < tend) __builtin_amdgcn_s_sleep(8);
  }
}
extern "C" void kernel_launch(void* const* d_in, const int* in_sizes, int n_in, void* d_out, int out_size, void* d_ws, size_t ws_size, hipStream_t stream) {
  (void)hipFuncSetAttribute((const void*)k_main, hipFuncAttributeMaxDynamicSharedMemorySize, L_END);
  k_main<<<256, 512, L_END, stream>>>((const char*)d_in[0], (const char*)d_in[1], (const char*)d_in[2], (const char*)d_in[3], (const char*)d_in[4], (char*)d_ws, (unsigned*)d_out);
  k_rep<<<1, 256, 0, stream>>>((const char*)d_ws);
}
```

```cpp
#include <hip/hip_runtime.h>
#include <hip/hip_bf16.h>
#include <type_traits>

typedef unsigned v4u __attribute__((ext_vector_type(4)));
typedef unsigned v2u __attribute__((ext_vector_type(2)));
typedef float f32x4 __attribute__((ext_vector_type(4)));
typedef float f32x2 __attribute__((ext_vector_type(2)));
typedef short bf16x8 __attribute__((ext_vector_type(8)));
typedef short s16x4 __attribute__((ext_vector_type(4)));
typedef __bf16 bf16x2 __attribute__((ext_vector_type(2)));
#define LAS __attribute__((address_space(3)))
#define GAS __attribute__((address_space(1)))

constexpr int DM = 448;
constexpr float C2 = 0.0625f * 1.4426950408889634f;
constexpr int RS = 4;
constexpr int RH = 4;
#define WLO 4
#define WMID 6
#define WHI 8
#define DUTY 8
#define TAU 1080
#define TOFF 60
#define PG 12
#define QSPIN 16
#define CROUNDS 4
#define CT0 2000
#define CSTEP 1200
#define CLAG 250
#define CODE_HI 4736
constexpr unsigned SPIN_CAP = 1u << 18, LSPIN_CAP = 1u << 24;

constexpr size_t W_REQ = 0;
constexpr size_t W_QG = 65536;
constexpr size_t W_INB = W_QG + 131072;
constexpr size_t W_CK = W_INB + 256 * 8192;

constexpr int L_RING = 0;
constexpr int L_SC = 4 * RS * 8192;
constexpr int L_Q = L_SC + 16384;
constexpr int L_ST = L_Q + 1024;
constexpr int L_RX = L_ST + 64;
constexpr int L_RS = L_RX + 2048;
constexpr int L_TAG = L_RS + 64;
constexpr int L_X = L_TAG + 64;
constexpr int L_END = L_X + 7168;

template <int N> __device__ __forceinline__ void waitvm() { asm volatile("s_waitcnt vmcnt(%0)" ::"i"(N) : "memory"); }
__device__ __forceinline__ void waitlgkm() { asm volatile("s_waitcnt lgkmcnt(0)" ::: "memory"); }
__device__ __forceinline__ void bar() { asm volatile("s_waitcnt lgkmcnt(0)\n\ts_barrier" ::: "memory"); }
__device__ __forceinline__ unsigned now() { return (unsigned)__builtin_amdgcn_s_memrealtime(); }
__device__ __forceinline__ void set_m0(unsigned v) { asm volatile("s_mov_b32 m0, %0\n\ts_nop 0" ::"s"(v) : "memory"); }
__device__ __forceinline__ void dma1(unsigned voff, const void* sbase) { asm volatile("global_load_lds_dwordx4 %0, %1" ::"v"(voff), "s"(sbase) : "memory"); }
#define BR3(p) ((((p) & 1) << 2) | ((p) & 2) | (((p) >> 2) & 1))
#define WMASK(d) ((1u << (d)) - 1u)
template <bool WAIT, int P, int OFF> __device__ __forceinline__ void piece(unsigned wm, unsigned voff, const void* sbase) {
  if constexpr (WAIT)
    asm volatile(
        "s_waitcnt vmcnt(%5)\n\t"
        "s_bitcmp1_b32 %0, %7\n\t"
        "s_cbranch_scc1 1f\n\t"
        "s_waitcnt vmcnt(%4)\n\t"
        "s_bitcmp1_b32 %0, %6\n\t"
        "s_cbranch_scc1 1f\n\t"
        "s_waitcnt vmcnt(%3)\n"
        "1:\n\t"
        "global_load_lds_dwordx4 %1, %2 offset:%8 nt" ::"s"(wm), "v"(voff), "s"(sbase), "i"(WLO - 1), "i"(WMID - 1), "i"(WHI - 1), "i"(BR3(P)), "i"(BR3(P) + 8), "i"(OFF)
        : "memory", "scc");
  else
    asm volatile("global_load_lds_dwordx4 %0, %1 offset:%2 nt" ::"v"(voff), "s"(sbase), "i"(OFF) : "memory");
}
__device__ __forceinline__ f32x4 mfma32(v4u a, v4u b, f32x4 c) {
  return __builtin_amdgcn_mfma_f32_16x16x32_bf16(__builtin_bit_cast(bf16x8, a), __builtin_bit_cast(bf16x8, b), c, 0, 0, 0);
}
__device__ __forceinline__ f32x4 mfma16(v2u a, v2u b, f32x4 c) {
  return __builtin_amdgcn_mfma_f32_16x16x16bf16_1k(__builtin_bit_cast(s16x4, a), __builtin_bit_cast(s16x4, b), c, 0, 0, 0);
}
__device__ __forceinline__ v2u ldtr(const LAS char* p) { return __builtin_bit_cast(v2u, __builtin_amdgcn_ds_read_tr16_b64_v4i16((LAS s16x4*)p)); }
__device__ __forceinline__ unsigned cvtpk(float lo, float hi) { return __builtin_bit_cast(unsigned, __builtin_convertvector(f32x2{lo, hi}, bf16x2)); }
__device__ __forceinline__ float dot2(unsigned a, unsigned b, float acc) {
  return __builtin_amdgcn_fdot2_f32_bf16(__builtin_bit_cast(bf16x2, a), __builtin_bit_cast(bf16x2, b), acc, false);
}
__device__ __forceinline__ float asf(unsigned u) { return __builtin_bit_cast(float, u); }
__device__ __forceinline__ unsigned asu(float f) { return __builtin_bit_cast(unsigned, f); }
__device__ __forceinline__ float ex2(float x) { return __builtin_amdgcn_exp2f(x); }
__device__ __forceinline__ float rows_max(float x) {
  auto s = __builtin_amdgcn_permlane16_swap(asu(x), asu(x), false, false);
  x = fmaxf(asf(s[0]), asf(s[1]));
  auto t = __builtin_amdgcn_permlane32_swap(asu(x), asu(x), false, false);
  return fmaxf(asf(t[0]), asf(t[1]));
}
__device__ __forceinline__ float rows_sum(float x) {
  auto s = __builtin_amdgcn_permlane16_swap(asu(x), asu(x), false, false);
  x = asf(s[0]) + asf(s[1]);
  auto t = __builtin_amdgcn_permlane32_swap(asu(x), asu(x), false, false);
  return asf(t[0]) + asf(t[1]);
}
template <int N> __device__ __forceinline__ unsigned row_bcast(unsigned x) { return (unsigned)__builtin_amdgcn_mov_dpp((int)x, 0x150 + N, 0xf, 0xf, true); }
#define RSRC(p, n) __builtin_amdgcn_make_buffer_rsrc((void*)(p), 0, (int)(n), 0x00020000)
constexpr int SC1 = 16;
__device__ __forceinline__ v4u poll128(__amdgpu_buffer_rsrc_t rs, int off) {
  asm volatile("" : "+v"(off)::"memory");
  return __builtin_amdgcn_raw_buffer_load_b128(rs, off, 0, SC1);
}
__device__ __forceinline__ v2u poll64(__amdgpu_buffer_rsrc_t rs, int off) {
  asm volatile("" : "+v"(off)::"memory");
  return __builtin_amdgcn_raw_buffer_load_b64(rs, off, 0, SC1);
}
__device__ __forceinline__ void idle_until(unsigned t) {
#pragma nounroll
  for (unsigned sp = 0; sp < 4096u && (int)(now() - t) < 0; ++sp) __builtin_amdgcn_s_sleep(16);
}
#define PINV(x) asm volatile("" : "+v"(x))

__device__ __forceinline__ void merge4(const LAS char* stp, const LAS char* pap, int pstride, float& Mx, float& Ls, float& o0, float& o1) {
  f32x2 st[4], pa[4];
#pragma unroll
  for (int w = 0; w < 4; ++w) {
    st[w] = *(const LAS f32x2*)(stp + w * 16);
    pa[w] = *(const LAS f32x2*)(pap + w * pstride);
  }
  Mx = fmaxf(fmaxf(st[0].x, st[1].x), fmaxf(st[2].x, st[3].x));
  Ls = 0.f; o0 = 0.f; o1 = 0.f;
#pragma unroll
  for (int w = 0; w < 4; ++w) {
    const float f = ex2(st[w].x - Mx);
    Ls = fmaf(f, st[w].y, Ls); o0 = fmaf(f, pa[w].x, o0); o1 = fmaf(f, pa[w].y, o1);
  }
}

__global__ void __launch_bounds__(512, 2) k_main(const char* x, const char* Kc, const char* Vc, const char* wq, const char* wo, char* ws, unsigned* out) {
  extern __shared__ __attribute__((aligned(16))) char lds_[];
  LAS char* lds = (LAS char*)lds_;
  const int tid = threadIdx.x, lane = tid & 63, wave = __builtin_amdgcn_readfirstlane(tid >> 6);
  const int bid = blockIdx.x, grp = bid >> 2, m = bid & 3, b = bid >> 5, jb = bid & 31, kvh = jb >> 2;
  const int g = lane >> 4, c = lane & 15, hd = lane >> 5;
  asm volatile("" : "+s"(x), "+s"(Kc), "+s"(Vc), "+s"(wq), "+s"(wo), "+s"(ws), "+s"(out));
  unsigned long long pc0;
  asm volatile("s_getpc_b64 %0" : "=s"(pc0));
  volatile LAS unsigned* ctl = (volatile LAS unsigned*)(lds + L_TAG + 8);
  if (tid < 6) ctl[tid] = 0u;
  if (__builtin_expect(wave < 4, 1)) {
    __builtin_amdgcn_s_setprio(3);
    const size_t t0 = (size_t)(bid >> 1) * 2097152 + (bid & 1) * 32768 + wave * 8192;
    const char* sbase = Kc + t0;
    unsigned step = grp & 31;
    const char* src = sbase + (size_t)step * 65536;
#define ADV() do { step = (step + 1) & 31; src = sbase + (size_t)step * 65536; } while (0)
    const unsigned ring0 = (unsigned)(size_t)lds + L_RING + wave * (RS * 8192);
    unsigned vo[8];
#pragma unroll
    for (int p = 0; p < 8; ++p) vo[p] = hd * 512 + ((((lane & 31) ^ hd) * 16) ^ (p << 5)) + (p >> 2) * 4096;
    unsigned wm = WMASK(DUTY);
#define ISW(W, p) do { if (((p) & 3) == 0) set_m0(dst + ((p) >> 2) * 4096); piece<W, (p), ((p) & 3) * 1024>(wm, vo[p], src); } while (0)
#define ISS(p) ISW(true, p)
    {
      const unsigned dst = ring0;
      ISW(false, 0); ISW(false, 1); ISW(false, 2); ISW(false, 3); ISW(false, 4); ISW(false, 5); ISW(false, 6); ISW(false, 7);
      ADV();
    }
    unsigned sched = now() * 10u + (unsigned)(TOFF * 10 + (RH + RS) * TAU);
    unsigned tnow = 0, adj = 0;
    bar();
#define PACE_STEP() do { tnow = now(); adj = ctl[1]; } while (0)
#define PACE_TAKE() do { const int late_ = (int)(tnow * 10u - sched); if (lane == 0) ctl[2 + wave] = (unsigned)late_; \
      int d_ = DUTY + (((late_ - (int)adj) * PG) / 1280); d_ = d_ < 0 ? 0 : d_ > 16 ? 16 : d_; wm = __builtin_amdgcn_readfirstlane(WMASK(d_)); sched += TAU; } while (0)
    const LAS char* wring = lds + L_RING + wave * (RS * 8192);
    LAS char* scw = lds + L_SC + wave * 4096 + (g * 2 + (c & 1)) * 16;
    const int kro = c * 512 + ((g ^ c) * 16);
#define KFRAG(base_, p) (*(const LAS v4u*)((base_) + ((kro ^ (((p) & 3) << 6)) + ((p) >> 2) * 256)))
    v4u hold[RH][8];
#pragma nounroll
    for (int r = 1;; ++r) {
      const int sl = r >= RS ? r - RS : r;
      if (r >= RS) {
        waitvm<8 * (RS - 1)>();
#pragma unroll
        for (int j = 0; j < RH; ++j)
          if (sl == j) {
#pragma unroll
            for (int p = 0; p < 8; ++p) hold[j][p] = KFRAG(wring + j * 8192, p);
          }
        waitlgkm();
        if (r == RS + RH - 1) break;
      }
      const unsigned dst = ring0 + sl * 8192;
      ISS(0); ISS(1); ISS(2); ISS(3); ISS(4); ISS(5); ISS(6); ISS(7);
      ADV();
    }
#pragma nounroll
    for (unsigned sp = 0; sp < LSPIN_CAP && ctl[0] == 0u; ++sp) __builtin_amdgcn_s_sleep(1);
    asm volatile("" ::: "memory");
    v4u qf[8];
    float mx = -INFINITY;
    f32x4 acc[16], accl;
    int fs = RH - 1, cs = RH % RS;
#define HOLD(r) do { if (FIRST) { f32x4 h = {0.f, 0.f, 0.f, 0.f}; _Pragma("unroll") for (int p = 0; p < 8; ++p) h = mfma32(hold[r][p], qf[p], h); \
      if (c < 2) *(LAS f32x4*)(scw + (r) * 128) = h; mx = fmaxf(fmaxf(mx, h[0]), fmaxf(fmaxf(h[1], h[2]), h[3])); PINV(mx); } } while (0)
    auto step_a = [&](auto first, int s) {
      constexpr bool FIRST = decltype(first)::value;
      waitvm<8 * (RS - 2)>();
      const LAS char* base = wring + cs * 8192;
      const unsigned dst = ring0 + fs * 8192;
      ISS(0);
      if (FIRST) {
#pragma unroll
        for (int p = 0; p < 8; ++p) qf[p] = *(const LAS v4u*)(lds + L_Q + (c & 1) * 512 + p * 64 + g * 16);
      }
      v4u kf[8];
#pragma unroll
      for (int p = 0; p < 8; ++p) kf[p] = KFRAG(base, p);
      f32x4 a = {0.f, 0.f, 0.f, 0.f};
      ISS(1);
      a = mfma32(kf[0], qf[0], a); a = mfma32(kf[1], qf[1], a); PINV(a);
      ISS(2);
      a = mfma32(kf[2], qf[2], a); a = mfma32(kf[3], qf[3], a); PINV(a);
      HOLD(0);
      ISS(3);
      a = mfma32(kf[4], qf[4], a); a = mfma32(kf[5], qf[5], a); PINV(a);
      HOLD(1);
      ISS(4);
      a = mfma32(kf[6], qf[6], a); a = mfma32(kf[7], qf[7], a); PINV(a);
      HOLD(2);
      ISS(5);
      if (c < 2) *(LAS f32x4*)(scw + s * 128) = a;
      mx = fmaxf(fmaxf(mx, a[0]), fmaxf(fmaxf(a[1], a[2]), a[3])); PINV(mx);
      HOLD(3);
      ISS(6);
      if (FIRST) {
        accl = f32x4{0.f, 0.f, 0.f, 0.f}; PINV(accl);
#pragma unroll
        for (int d = 0; d < 16; ++d) { acc[d] = f32x4{0.f, 0.f, 0.f, 0.f}; PINV(acc[d]); }
      }
      PACE_STEP();
      ISS(7);
      PACE_TAKE();
      ADV();
      fs = cs; cs = (cs == RS - 1) ? 0 : cs + 1;
    };
    step_a(std::true_type{}, RH);
#pragma nounroll
    for (int s = RH + 1; s < 32; ++s) {
      if (s == 33 - RS) {
        sbase = Vc + t0; src = sbase + (size_t)step * 65536;
#pragma unroll
        for (int p = 0; p < 8; ++p) vo[p] = hd * 512 + ((((lane & 31) ^ (hd << 1)) * 16) ^ ((p & 3) << 6)) + (p >> 2) * 4096;
      }
      step_a(std::false_type{}, s);
    }
    const float M = rows_max(mx) * C2;
    const int kq = 4 * g + ((lane >> 2) & 3);
    const int vro = kq * 512 + (kq & 7) * 32 + (lane & 3) * 8;
    const v2u ones = {0x3F803F80u, 0x3F803F80u};
#define PFRAG(sc_) v2u{cvtpk(ex2(fmaf(sc_[0], C2, -M)), ex2(fmaf(sc_[1], C2, -M))), cvtpk(ex2(fmaf(sc_[2], C2, -M)), ex2(fmaf(sc_[3], C2, -M)))}
#define PVM(p) do { acc[p] = mfma16(vf[p], pf, acc[p]); acc[(p) + 8] = mfma16(vf[(p) + 8], pf, acc[(p) + 8]); asm volatile("" : "+v"(acc[p]), "+v"(acc[(p) + 8])); } while (0)
#pragma nounroll
    for (int i = 0; i < 32 - RS; ++i) {
      waitvm<8 * (RS - 2)>();
      const LAS char* base = wring + cs * 8192;
      const unsigned dst = ring0 + fs * 8192;
      ISS(0);
      const f32x4 sc = *(const LAS f32x4*)(scw + i * 128);
      v2u vf[16];
#pragma unroll
      for (int p = 0; p < 4; ++p) { const LAS char* a = base + (vro ^ (p << 5)); vf[p] = ldtr(a); vf[p + 8] = ldtr(a + 256); }
      ISS(1);
#pragma unroll
      for (int p = 4; p < 8; ++p) { const LAS char* a = base + (vro ^ (p << 5)); vf[p] = ldtr(a); vf[p + 8] = ldtr(a + 256); }
      v2u pf = PFRAG(sc);
      PINV(pf);
      ISS(2);
      accl = mfma16(ones, pf, accl);
      PVM(0); PVM(1);
      ISS(3);
      PVM(2); PVM(3);
      ISS(4);
      PVM(4); PVM(5);
      ISS(5);
      PVM(6); PVM(7);
      ISS(6);
      PACE_STEP();
      ISS(7);
      PACE_TAKE();
      ADV();
      fs = cs; cs = (cs == RS - 1) ? 0 : cs + 1;
    }
    {
      const unsigned dst = ring0 + fs * 8192;
      ISW(false, 0); ISW(false, 1); ISW(false, 2); ISW(false, 3); ISW(false, 4); ISW(false, 5); ISW(false, 6); ISW(false, 7);
    }
#pragma nounroll
    for (int i = 32 - RS; i < 32; ++i) {
      const LAS char* base = wring + cs * 8192;
      const f32x4 sc = *(const LAS f32x4*)(scw + i * 128);
      v2u pf = PFRAG(sc);
      accl = mfma16(ones, pf, accl);
      asm volatile("" : "+v"(pf), "+v"(accl));
      if (i == 31) waitvm<0>(); else if (i == 30) waitvm<8>(); else waitvm<16>();
#pragma unroll
      for (int p = 0; p < 8; ++p) {
        const LAS char* a = base + (vro ^ (p << 5));
        acc[p] = mfma16(ldtr(a), pf, acc[p]);
        acc[p + 8] = mfma16(ldtr(a + 256), pf, acc[p + 8]);
      }
      cs = (cs == RS - 1) ? 0 : cs + 1;
    }
    waitlgkm();
    if (c < 2) {
#pragma unroll
      for (int d = 0; d < 16; ++d) *(LAS f32x4*)(lds + L_RING + wave * (RS * 8192) + c * 1024 + (16 * d + 4 * g) * 4) = acc[d];
      if (g == 0) *(LAS f32x2*)(lds + L_ST + wave * 16 + c * 8) = f32x2{M, accl[0]};
    }
    bar();
    const v2u tag = *(const LAS v2u*)(lds + L_TAG);
    {
      float Mx, Ls, o0, o1;
      merge4(lds + L_ST + hd * 8, lds + L_RING + hd * 1024 + wave * 256 + (lane & 31) * 8, RS * 8192, Mx, Ls, o0, o1);
      if (wave != m) {
        const __amdgpu_buffer_rsrc_t ro = RSRC(ws + W_INB + (size_t)(grp * 4 + wave) * 8192 + 4096 + (((m - wave) & 3) - 1) * 1152, 1152);
        __builtin_amdgcn_raw_buffer_store_b128(v4u{asu(o0), tag.x, asu(o1), tag.y}, ro, lane * 16, 0, SC1);
        if ((lane & 31) == 0) __builtin_amdgcn_raw_buffer_store_b128(v4u{asu(Mx), tag.x, asu(Ls), tag.y}, ro, 1024 + hd * 16, 0, SC1);
        const __amdgpu_buffer_rsrc_t ri = RSRC(ws + W_INB + (size_t)bid * 8192 + 4096 + (((wave - m) & 3) - 1) * 1152, 1152);
        v4u v, s;
#pragma nounroll
        for (unsigned sp = 0; sp < SPIN_CAP; ++sp) {
          v = poll128(ri, lane * 16);
          s = poll128(ri, 1024 + hd * 16);
          if (__all((v.y == tag.x) & (v.w == tag.y) & (s.y == tag.x) & (s.w == tag.y))) break;
        }
        o0 = asf(v.x); o1 = asf(v.z); Mx = asf(s.x); Ls = asf(s.z);
      } else {
        const v2u u = poll64(RSRC(ws + W_INB + (size_t)bid * 8192, 4096), lane * 64);
        asm volatile("" ::"v"(u));
      }
      *(LAS f32x2*)(lds + L_RX + wave * 512 + lane * 8) = f32x2{o0, o1};
      if ((lane & 31) == 0) *(LAS f32x2*)(lds + L_RS + wave * 16 + hd * 8) = f32x2{Mx, Ls};
    }
    bar();
    if (wave == 0 && jb < 28) {
      const __amdgpu_buffer_rsrc_t rs = RSRC(ws + W_INB + (size_t)bid * 8192, 4096);
      v4u v[4];
      __builtin_amdgcn_s_sleep(32);
#pragma nounroll
      for (unsigned sp = 0; sp < SPIN_CAP; ++sp) {
#pragma unroll
        for (int it = 0; it < 4; ++it) v[it] = poll128(rs, it * 1024 + lane * 16);
        bool ok = true;
#pragma unroll
        for (int it = 0; it < 4; ++it) ok &= (v[it].y == tag.x) & (v[it].w == tag.y);
        if (__all(ok)) break;
      }
      float s0 = (asf(v[0].x) + asf(v[1].x)) + (asf(v[2].x) + asf(v[3].x));
      float s1 = (asf(v[0].z) + asf(v[1].z)) + (asf(v[2].z) + asf(v[3].z));
      s0 += asf(__builtin_amdgcn_mov_dpp((int)asu(s0), 0x128, 0xf, 0xf, true));
      s1 += asf(__builtin_amdgcn_mov_dpp((int)asu(s1), 0x128, 0xf, 0xf, true));
      s0 = rows_sum(s0); s1 = rows_sum(s1);
      if (lane < 8) __builtin_amdgcn_raw_buffer_store_b32(cvtpk(s0, s1), RSRC(out, 8 * DM * 2), (b * (DM / 2) + jb * 8 + lane) * 4, 0, 17);
    }
  } else {
    const int hw = wave - 4;
    bar();
    const unsigned tk0 = now();
    if (hw == 0) {
      const int cb = (bid & 7) * 32 + (bid >> 3);
      const __amdgpu_buffer_rsrc_t rq = RSRC(ws + W_QG, 131072);
      const int qin = b * 16384 + kvh * 2048 + lane * 16, qout = (c & 7) * 16384 + (cb * 4 + g) * 16;
      v4u o0 = poll128(rq, qin), o1 = poll128(rq, qin + 1024), mine = poll128(rq, qout);
      const unsigned l0 = (unsigned)(size_t)lds;
      const unsigned voff = (lane >> 1) * 8192 + (lane & 1) * 16;
      const LAS char* lp = lds + L_SC + (8 * g + ((lane >> 2) & 3)) * 32 + (lane & 3) * 8;
      const LAS char* lx = lds + L_X + (c & 7) * (DM * 2) + g * 16;
      unsigned long long miss0 = 0, miss1 = 0;
      int blk = cb;
#pragma nounroll
      for (int pass = 0;; ++pass) {
        const char* wsrc = wq + blk * 32;
#pragma nounroll
        for (int s = 0; s < 14; ++s) { set_m0(l0 + L_SC + s * 1024); dma1(voff, wsrc); wsrc += 32 * 8192; }
        if (pass == 0) {
#pragma nounroll
          for (int s = 0; s < 7; ++s) { set_m0(l0 + L_X + s * 1024); dma1(lane * 16, x + s * 1024); }
        }
        asm volatile("s_waitcnt vmcnt(0)" : "+v"(o0), "+v"(o1), "+v"(mine)::"memory");
        f32x4 qa = {0.f, 0.f, 0.f, 0.f}, qb = {0.f, 0.f, 0.f, 0.f};
#pragma nounroll
        for (int s = 0; s < 14; s += 2) {
          const v2u a0 = ldtr(lp + s * 1024), a1 = ldtr(lp + s * 1024 + 128), b0 = ldtr(lp + s * 1024 + 1024), b1 = ldtr(lp + s * 1024 + 1152);
          qa = mfma32(v4u{a0.x, a0.y, a1.x, a1.y}, *(const LAS v4u*)(lx + s * 64), qa);
          qb = mfma32(v4u{b0.x, b0.y, b1.x, b1.y}, *(const LAS v4u*)(lx + s * 64 + 64), qb);
        }
        qa += qb;
        const v2u qq = {cvtpk(qa[0], qa[1]), cvtpk(qa[2], qa[3])};
        if (pass == 0) {
          if (c < 8) __builtin_amdgcn_raw_buffer_store_b128(v4u{qq.x, mine.y + 1u, qq.y, mine.w + 1u}, rq, qout, 0, SC1);
          v4u v0, v1;
#pragma nounroll
          for (unsigned sp = 0;; ++sp) {
            __builtin_amdgcn_s_sleep(8);
            v0 = poll128(rq, qin);
            v1 = poll128(rq, qin + 1024);
            if (sp >= QSPIN || __all((v0.y != o0.y) & (v0.w != o0.w) & (v1.y != o1.y) & (v1.w != o1.w))) break;
          }
          *(LAS v2u*)(lds + L_Q + lane * 8) = v2u{v0.x, v0.z};
          *(LAS v2u*)(lds + L_Q + 512 + lane * 8) = v2u{v1.x, v1.z};
          miss0 = __ballot((v0.y == o0.y) | (v0.w == o0.w));
          miss1 = __ballot((v1.y == o1.y) | (v1.w == o1.w));
        } else {
          if (c == b) *(LAS v2u*)(lds + L_Q + (blk & 31) * 32 + g * 8) = qq;
        }
        if (miss0) { const int j = __builtin_ctzll(miss0) >> 2; miss0 &= ~(15ull << (4 * j)); blk = kvh * 32 + j; }
        else if (miss1) { const int j = __builtin_ctzll(miss1) >> 2; miss1 &= ~(15ull << (4 * j)); blk = kvh * 32 + 16 + j; }
        else break;
        waitlgkm();
      }
      waitlgkm();
      if (lane == 0) ctl[0] = 1u;
    } else {
#pragma nounroll
      for (unsigned sp = 0; sp < LSPIN_CAP && ctl[0] == 0u; ++sp) __builtin_amdgcn_s_sleep(16);
    }
    if (hw == 1) {
      const unsigned long long clk = __builtin_amdgcn_s_memrealtime();
      const unsigned n0 = (unsigned)clk, n1 = (unsigned)(clk >> 8) & ~1u;
      const __amdgpu_buffer_rsrc_t rr = RSRC(ws + W_REQ + (size_t)bid * 256, 256);
      if (lane == 0) __builtin_amdgcn_raw_buffer_store_b64(v2u{n0, n1}, rr, 0, 0, SC1);
      v4u rep = {0u, 0u, 0u, 0u};
#pragma nounroll
      for (unsigned sp = 0; sp < SPIN_CAP; ++sp) {
        __builtin_amdgcn_s_sleep(64);
        rep = poll128(rr, 128);
        if (__builtin_amdgcn_readfirstlane(rep.y) == n0 && __builtin_amdgcn_readfirstlane(rep.w) == n1) break;
      }
      if (lane == 0) {
        __builtin_amdgcn_raw_buffer_store_b64(v2u{rep.x, rep.z}, rr, 0, 0, SC1);
        *(LAS v2u*)(lds + L_TAG) = v2u{rep.x, rep.z};
      }
    } else if (hw == 2 && jb == 0) {
      const unsigned long long clk = __builtin_amdgcn_s_memrealtime();
      const unsigned t0 = (unsigned)clk, t1 = (unsigned)(clk >> 8) | 1u;
      const __amdgpu_buffer_rsrc_t rl = RSRC(ws + W_REQ + (size_t)bid * 256, 32 * 256);
      v2u last = {t0, t1};
#pragma nounroll
      for (unsigned sp = 0; sp < SPIN_CAP; ++sp) {
        __builtin_amdgcn_s_sleep(32);
        const v2u r = poll64(rl, (lane & 31) * 256);
        const bool d = (r.x == t0) & (r.y == t1);
        const bool plaus = (sp >= 64) | (r.x - t0 + 0x100000u < 0x200000u);
        if (lane < 32 && !d && plaus && (r.x != last.x || r.y != last.y)) {
          __builtin_amdgcn_raw_buffer_store_b128(v4u{t0, r.x, t1, r.y}, rl, lane * 256 + 128, 0, SC1);
          last = r;
        }
        if (__all(d)) break;
      }
    }
    const int ch = c < 14 ? c : 13;
    const char* wp = wo + (size_t)((2 * kvh + (g >> 1)) * 256 + 64 * m + 32 * (g & 1)) * (DM * 2) + hw * 224 + ch * 16;
    unsigned wr[16][8];
#pragma unroll
    for (int i0 = 0; i0 < 16; i0 += 2) {
      v4u r[4];
#pragma unroll
      for (int u = 0; u < 4; ++u) r[u] = *(const GAS v4u*)(wp + (size_t)(2 * i0 + u) * (DM * 2));
#pragma unroll
      for (int u = 0; u < 2; ++u) {
#pragma unroll
        for (int e = 0; e < 4; ++e) {
          wr[i0 + u][2 * e] = __builtin_amdgcn_perm(r[2 * u + 1][e], r[2 * u][e], 0x05040100u);
          wr[i0 + u][2 * e + 1] = __builtin_amdgcn_perm(r[2 * u + 1][e], r[2 * u][e], 0x07060302u);
        }
        asm volatile("" : "+v"(wr[i0 + u][0]), "+v"(wr[i0 + u][1]), "+v"(wr[i0 + u][2]), "+v"(wr[i0 + u][3]), "+v"(wr[i0 + u][4]), "+v"(wr[i0 + u][5]), "+v"(wr[i0 + u][6]), "+v"(wr[i0 + u][7])::"memory");
      }
    }
    if (hw == 3) {
      const __amdgpu_buffer_rsrc_t rc = RSRC(ws + W_CK, 64 * 4352);
#pragma nounroll
      for (int k = 0; k < CROUNDS; ++k) {
        const unsigned tgo = tk0 + CT0 + k * CSTEP;
        idle_until(tgo);
        if (bid % 17 == k && lane < 4) {
          const int l4 = ((int)ctl[2] + (int)ctl[3] + (int)ctl[4] + (int)ctl[5]) >> 2;
          __builtin_amdgcn_raw_buffer_store_b64(v2u{(unsigned)l4, tgo}, rc, (k * 4 + lane) * 4352 + (bid / 17) * 8, 0, SC1);
        }
        idle_until(tgo + CLAG);
        const v2u r = poll64(rc, (k * 4 + ((bid >> 3) & 3)) * 4352 + c * 8);
        const bool ok = (r.y - tgo + 500u < 1000u) & ((int)r.x > -20000) & ((int)r.x < 20000);
        float sum = ok ? (float)(int)r.x : 0.f, cnt = ok ? 1.f : 0.f;
#pragma unroll
        for (int o = 1; o < 16; o <<= 1) { sum += __shfl_xor(sum, o); cnt += __shfl_xor(cnt, o); }
        if (cnt >= 8.f && lane == 0) ctl[1] = (unsigned)(int)(sum / cnt);
      }
    }
    if (hw == 2) {
      idle_until(tk0 + 6400);
      const GAS char* cp = (const GAS char*)(pc0 & ~127ull) + lane * 128;
      const unsigned t0 = *(const GAS unsigned*)cp, t1 = *(const GAS unsigned*)(cp + CODE_HI);
      asm volatile("" ::"v"(t0), "v"(t1));
    }
    bar();
    const v2u tag = *(const LAS v2u*)(lds + L_TAG);
    bar();
    float Mx, Ls, o0, o1;
    merge4(lds + L_RS + hd * 8, lds + L_RX + lane * 8, 512, Mx, Ls, o0, o1);
    const float inv = __builtin_amdgcn_rcpf(Ls);
    const unsigned op = cvtpk(o0 * inv, o1 * inv);
    float y[8];
#pragma unroll
    for (int e = 0; e < 8; ++e) y[e] = 0.f;
#define STEP(i) { const unsigned ob = row_bcast<i>(op); _Pragma("unroll") for (int e = 0; e < 8; ++e) y[e] = dot2(wr[i][e], ob, y[e]); }
    STEP(0) STEP(1) STEP(2) STEP(3) STEP(4) STEP(5) STEP(6) STEP(7) STEP(8) STEP(9) STEP(10) STEP(11) STEP(12) STEP(13) STEP(14) STEP(15)
#undef STEP
    float z[4];
#pragma unroll
    for (int e = 0; e < 4; ++e) { auto s = __builtin_amdgcn_permlane32_swap(asu(y[e]), asu(y[e + 4]), false, false); z[e] = asf(s[0]) + asf(s[1]); }
    float u[2];
#pragma unroll
    for (int e = 0; e < 2; ++e) { auto s = __builtin_amdgcn_permlane16_swap(asu(z[e]), asu(z[e + 2]), false, false); u[e] = asf(s[0]) + asf(s[1]); }
    if (c < 14) {
      const int n0 = hw * 112 + c * 8 + g * 2;
      const __amdgpu_buffer_rsrc_t ri = RSRC(ws + W_INB + (size_t)b * (32 * 8192), 32 * 8192);
      __builtin_amdgcn_raw_buffer_store_b128(v4u{asu(u[0]), tag.x, asu(u[1]), tag.y}, ri, (n0 >> 4) * 8192 + jb * 128 + (n0 & 15) * 8, 0, SC1);
    }
  }
}

extern "C" void kernel_launch(void* const* d_in, const int* in_sizes, int n_in, void* d_out, int out_size, void* d_ws, size_t ws_size, hipStream_t stream) {
  (void)hipFuncSetAttribute((const void*)k_main, hipFuncAttributeMaxDynamicSharedMemorySize, L_END);
  k_main<<<256, 512, L_END, stream>>>((const char*)d_in[0], (const char*)d_in[1], (const char*)d_in[2], (const char*)d_in[3], (const char*)d_in[4], (char*)d_ws, (unsigned*)d_out);
}
```
